```python
import torch

def _softmax(x, axis=-1):
    return torch.softmax(x, dim=axis)

def _gelu(x):
    return torch.nn.functional.gelu(x, approximate='tanh')

def _silu(x):
    return torch.nn.functional.silu(x)

def _sigmoid(x):
    return torch.sigmoid(x)

def _layernorm(x, axis=-1, eps=1e-05):
    m = x.mean(dim=axis, keepdim=True)
    v = x.var(dim=axis, keepdim=True, unbiased=False)
    return (x - m) / torch.sqrt(v + eps)

def _scatter_add(tgt, idx, src, axis=0):
    return tgt.clone().index_add_(axis, idx, src)

def _segment_sum(src, seg, n):
    out = torch.zeros((n,) + src.shape[1:], dtype=src.dtype)
    return out.index_add_(0, seg, src)

def _pad(x, pw):
    flat = tuple((v for pair in reversed(pw) for v in pair))
    return torch.nn.functional.pad(x, flat)
kernel_name = 'gated_mix__memory_aug_recurrent_mem_tr__dense_activation_silu__prompt'

def setup_inputs():

    _w = lambda a: (lambda t: t.to(torch.bfloat16) if t.is_floating_point() else t)(torch.as_tensor(a))

    def _A_setup():
        B, S, Dm = (1, 2048, 512)
        H, Dh = (32, 128)
        M, seg = (8, 32)
        seg = min(seg, S)
        S = S // seg * seg
        return {'x': _w((lambda *s: torch.randn(*s, dtype=torch.bfloat16) * 0.05)(B, S, Dm)), 'mem0': _w((lambda *s: torch.randn(*s, dtype=torch.bfloat16) * 0.4)(B, M, Dm)), 'Wq': _w((lambda *s: torch.randn(*s, dtype=torch.bfloat16) * 6.4)(Dm, H, Dh) * Dm ** (-0.5)), 'Wk': _w((lambda *s: torch.randn(*s, dtype=torch.bfloat16) * 6.4)(Dm, H, Dh) * Dm ** (-0.5)), 'Wv': _w((lambda *s: torch.randn(*s, dtype=torch.bfloat16) * 0.4)(Dm, H, Dh) * Dm ** (-0.5)), 'Wo': _w((lambda *s: torch.randn(*s, dtype=torch.bfloat16) * 0.4)(H, Dh, Dm) * (H * Dh) ** (-0.5)), 'seg_len': seg}
    _A_in = _A_setup()

    def _B_setup():
        B, S, D, F = (1, 2048, 512, 1792)
        return {'x': _w((lambda *s: torch.randn(*s, dtype=torch.bfloat16) * 0.05)(B, S, D)), 'w1': _w((lambda *s: torch.randn(*s, dtype=torch.bfloat16) * 3.2)(D, F) * D ** (-0.5)), 'w2': _w((lambda *s: torch.randn(*s, dtype=torch.bfloat16) * 0.2)(F, D) * F ** (-0.5))}
    _B_in = _B_setup()
    return {'A_x': _A_in['x'], 'A_mem0': _A_in['mem0'], 'A_Wq': _A_in['Wq'], 'A_Wk': _A_in['Wk'], 'A_Wv': _A_in['Wv'], 'A_Wo': _A_in['Wo'], 'A_seg_len': _A_in['seg_len'], 'B_x': _B_in['x'], 'B_w1': _B_in['w1'], 'B_w2': _B_in['w2'], 'gate_w': _w((lambda *s: torch.randn(*s, dtype=torch.bfloat16) * 0.05)(512) * 16.0)}

def reference(A_x, A_mem0, A_Wq, A_Wk, A_Wv, A_Wo, A_seg_len, B_x, B_w1, B_w2, gate_w):

    def _A_ref(x, mem0, Wq, Wk, Wv, Wo, seg_len):
        B, S, Dm = x.shape
        H, Dh = (Wq.shape[1], Wq.shape[2])
        M = mem0.shape[1]
        scale = Dh ** (-0.5)
        n_seg = S // seg_len
        L = M + seg_len
        idx = torch.arange(L)
        mask = (idx[:, None] < M) | (idx[:, None] >= idx[None, :])
        mem = mem0
        outs = []
        for s in range(n_seg):
            xs = x[:, s * seg_len:(s + 1) * seg_len, :].clone()
            h = torch.concatenate([mem, xs], axis=1)
            q = torch.einsum('btd,dhe->bhte', h, Wq)
            k = torch.einsum('btd,dhe->bhte', h, Wk)
            v = torch.einsum('btd,dhe->bhte', h, Wv)
            scores = torch.einsum('bhqe,bhke->bhqk', q, k) * scale
            scores = torch.where(mask[None, None], scores, -1000000000.0)
            p = _softmax(scores, axis=-1)
            o = torch.einsum('bhqk,bhke->bhqe', p, v)
            o = torch.einsum('bhte,hed->btd', o, Wo)
            mem = o[:, :M, :].clone()
            outs.append(o[:, M:, :])
        return torch.concatenate(outs, axis=1)

    def _B_ref(x, w1, w2):
        h = torch.einsum('bsd,df->bsf', x, w1)
        h = _silu(h)
        out = torch.einsum('bsf,fd->bsd', h, w2)
        return out
    x = A_x
    g = _sigmoid((x * gate_w).sum(-1, keepdims=True))
    a = _A_ref(x, A_mem0, A_Wq, A_Wk, A_Wv, A_Wo, A_seg_len)
    b = _B_ref(x, B_w1, B_w2)
    return g * a + (1 - g) * b
```

```cpp
#include <hip/hip_runtime.h>
#include <hip/hip_bf16.h>
#include <stdint.h>

#define GAS __attribute__((address_space(1)))
#define LAS __attribute__((address_space(3)))
typedef unsigned short u16;
typedef unsigned int u32;
typedef u32 v4u __attribute__((ext_vector_type(4)));
typedef u32 v2u __attribute__((ext_vector_type(2)));
typedef u32 v8u __attribute__((ext_vector_type(8)));
typedef float f32x4 __attribute__((ext_vector_type(4)));
typedef float f32x2 __attribute__((ext_vector_type(2)));
typedef short s16x4 __attribute__((ext_vector_type(4)));
typedef __bf16 bf16x2 __attribute__((ext_vector_type(2)));
typedef __bf16 bf16x8 __attribute__((ext_vector_type(8)));
typedef __bf16 bf16x16 __attribute__((ext_vector_type(16)));

constexpr int SEQ = 2048, DM = 512, NH = 32, DH = 128, NSEG = 64, FF = 1792, HD = NH * DH;
constexpr u32 SENT = 0x7F817F81u;
constexpr int RSRC_W3 = 0x00020000;
constexpr int SC1 = 16;
constexpr float SCALE_LOG2E = 0.08838834764831845f * 1.4426950408889634f;
constexpr float MASKED = -999292928.0f;
constexpr u32 SPIN_CAP = 1u << 18;

constexpr size_t MB = 1024 * 1024;
constexpr size_t WS_WIN = 0;
constexpr size_t WS_CTRL = 2 * MB;
constexpr size_t WS_KF = 3 * MB;
constexpr size_t WS_QF = WS_KF + 16 * MB;
constexpr size_t WS_VF = WS_QF + 16 * MB;
constexpr size_t WS_OF = WS_VF + 16 * MB;
constexpr size_t WS_HF = WS_OF + 16 * MB;
constexpr size_t WS_KM = WS_HF + 7 * MB;
constexpr size_t WS_VM = WS_KM + 4 * MB;
constexpr size_t WS_END = WS_VM + 4 * MB;
constexpr int WIN_PART = 0;
constexpr int WIN_MEM = 512 * 1024;
constexpr int CTRL_PAR = 0;
constexpr int CTRL_TMO = 64;

struct Params {
  const u16 *x, *mem0, *wq, *wk, *wv, *wo, *w1, *w2, *gw;
  u16* out;
  unsigned char* ws;
};

__device__ __forceinline__ u32 pk2(float a, float b) {
  f32x2 v = {a, b};
  bf16x2 r = __builtin_convertvector(v, bf16x2);
  return __builtin_bit_cast(u32, r);
}
__device__ __forceinline__ float bflo(u32 u) { return __uint_as_float(u << 16); }
__device__ __forceinline__ float bfhi(u32 u) { return __uint_as_float(u & 0xFFFF0000u); }
__device__ __forceinline__ float rbf(float a) { return bflo(pk2(a, a) & 0xFFFFu); }
__device__ __forceinline__ f32x4 mfma32(v4u a, v4u b, f32x4 c) {
  return __builtin_amdgcn_mfma_f32_16x16x32_bf16(__builtin_bit_cast(bf16x8, a), __builtin_bit_cast(bf16x8, b), c, 0, 0, 0);
}
__device__ __forceinline__ f32x4 mfma16(v2u a, v2u b, f32x4 c) {
  return __builtin_amdgcn_mfma_f32_16x16x16bf16_1k(__builtin_bit_cast(s16x4, a), __builtin_bit_cast(s16x4, b), c, 0, 0, 0);
}
__device__ __forceinline__ f32x4 smf(v4u a, v8u b, f32x4 c, int idx) {
  return __builtin_amdgcn_smfmac_f32_16x16x64_bf16(__builtin_bit_cast(bf16x8, a), __builtin_bit_cast(bf16x16, b), c, idx, 0, 0);
}
__device__ __forceinline__ f32x4 vzero() {
  f32x4 z = {0.f, 0.f, 0.f, 0.f};
  asm volatile("" : "+v"(z));
  return z;
}
__device__ __forceinline__ void mfma32_v0(f32x4& d, v4u a, v4u b) { asm volatile("v_mfma_f32_16x16x32_bf16 %0, %1, %2, 0" : "=&v"(d) : "v"(a), "v"(b)); }
__device__ __forceinline__ void mfma32_va(f32x4& d, v4u a, v4u b) { asm volatile("v_mfma_f32_16x16x32_bf16 %0, %1, %2, %0" : "+v"(d) : "v"(a), "v"(b)); }
__device__ __forceinline__ void mfma16_va(f32x4& d, v2u a, v2u b) { asm volatile("v_mfma_f32_16x16x16_bf16 %0, %1, %2, %0" : "+v"(d) : "v"(a), "v"(b)); }
__device__ __forceinline__ v2u trread(u32 lds_addr) {
  s16x4 v = __builtin_amdgcn_ds_read_tr16_b64_v4i16((LAS s16x4*)lds_addr);
  return __builtin_bit_cast(v2u, v);
}
__device__ __forceinline__ float fold32(float a, float b) {
  auto r = __builtin_amdgcn_permlane32_swap(__float_as_uint(a), __float_as_uint(b), false, false);
  u32 r0 = r[0], r1 = r[1];
  return __uint_as_float(r0) + __uint_as_float(r1);
}
__device__ __forceinline__ float fold16(float a, float b) {
  auto r = __builtin_amdgcn_permlane16_swap(__float_as_uint(a), __float_as_uint(b), false, false);
  u32 r0 = r[0], r1 = r[1];
  return __uint_as_float(r0) + __uint_as_float(r1);
}
__device__ __forceinline__ float max_g(float v) {
  auto r = __builtin_amdgcn_permlane16_swap(__float_as_uint(v), __float_as_uint(v), false, false);
  u32 r0 = r[0], r1 = r[1];
  v = fmaxf(__uint_as_float(r0), __uint_as_float(r1));
  auto q = __builtin_amdgcn_permlane32_swap(__float_as_uint(v), __float_as_uint(v), false, false);
  u32 q0 = q[0], q1 = q[1];
  return fmaxf(__uint_as_float(q0), __uint_as_float(q1));
}
__device__ __forceinline__ float sum_g(float v) { v = fold16(v, v); return fold32(v, v); }
__device__ __forceinline__ float ror4(float v) { return __int_as_float(__builtin_amdgcn_update_dpp(0, __float_as_int(v), 0x124, 0xf, 0xf, true)); }
__device__ __forceinline__ float ror8(float v) { return __int_as_float(__builtin_amdgcn_update_dpp(0, __float_as_int(v), 0x128, 0xf, 0xf, true)); }
__device__ __forceinline__ void glds16(const void* gsrc, u32 lds_dst) {
  u32 keep;
  asm volatile("s_mov_b32 %0, m0\n\ts_mov_b32 m0, %2\n\ts_nop 0\n\tglobal_load_lds_dwordx4 %1, off\n\ts_mov_b32 m0, %0"
               : "=&s"(keep) : "v"(gsrc), "s"(lds_dst) : "memory");
}
#define WAIT_VM(n) asm volatile("s_waitcnt vmcnt(" #n ")" ::: "memory")
#define WAIT_LGKM0() asm volatile("s_waitcnt lgkmcnt(0)" ::: "memory")
#define BARRIER() __builtin_amdgcn_s_barrier()
__device__ __forceinline__ int wave_id() { return __builtin_amdgcn_readfirstlane(threadIdx.x >> 6); }
__device__ __forceinline__ u32 lds_base(unsigned char* smem) { return (u32)(uintptr_t)(LAS unsigned char*)smem; }
__device__ __forceinline__ int remap(int a, int par) { return ((a >> 13) << 14) + (par << 13) + (a & 8191); }

__global__ __launch_bounds__(256) void k_init(Params p) {
  v4u* win = (v4u*)(p.ws + WS_WIN);
  const v4u s4 = {SENT, SENT, SENT, SENT};
  const int n = (int)((2 * MB - 32768) / 16);
  for (int i = blockIdx.x * 256 + threadIdx.x; i < n; i += gridDim.x * 256) {
    v4u v = s4;
    if ((i >> 12) == 2 * WIN_MEM / 65536) {
      const int ks = (i >> 6) & 7, g = (i >> 4) & 3, r = i & 15;
      const u16* m0 = p.mem0 + (r & 7) * DM + 64 * ks + 32 * (g >> 1) + 8 * (g & 1) + 4 * (r >> 3);
#pragma unroll
      for (int pq = 0; pq < 4; ++pq) {
        v[pq] = (u32)m0[pq] | ((u32)m0[pq + 16] << 16);
        if (v[pq] == SENT) v[pq] = 0x7FC17FC1u;
      }
    }
    win[i] = v;
  }
  if (blockIdx.x == 0 && threadIdx.x < 64) {
    const __amdgpu_buffer_rsrc_t rs = __builtin_amdgcn_make_buffer_rsrc((void*)(p.ws + WS_WIN), 0, (int)(2 * MB), RSRC_W3);
    u32 best[2] = {1u << 30, 1u << 30};
    for (int it = 0; it < 4; ++it)
#pragma unroll
      for (int par = 0; par < 2; ++par) {
        const unsigned long long a = __builtin_amdgcn_s_memtime();
        if (threadIdx.x < 8) __builtin_amdgcn_raw_buffer_store_b128(s4, rs, (int)(2 * MB - 32768) + par * 8192 + threadIdx.x * 16, 0, SC1);
        WAIT_VM(0);
        const u32 d = (u32)(__builtin_amdgcn_s_memtime() - a);
        best[par] = min(best[par], d);
      }
    if (threadIdx.x == 0) {
      u32* ctrl = (u32*)(p.ws + WS_CTRL);
      ctrl[CTRL_PAR] = best[1] < best[0] ? 1u : 0u;
      ctrl[CTRL_TMO] = 0u;
    }
  }
}

__device__ __forceinline__ int wswz(int k) { return (k & 3) | ((((k >> 2) ^ (k >> 3)) & 1) << 2); }

template <int NPW, bool AF, bool WFIRST>
__device__ __forceinline__ void gemm_core(f32x4 (&acc)[8][NPW], u32 lds, const u16* act, int act_ld  ,
                                          const u16* wsrc0, const u16* wsrc1  , int ldw, int K) {
  constexpr int NT = 64 * NPW, RB = NT * 2, WBYTES = 64 * RB, STAGE = 16384 + WBYTES;
  constexpr int WI = WBYTES / 4096;
  constexpr int RPI = 1024 / RB;
  constexpr int CPR = RB / 16;
  const int lane = threadIdx.x & 63, w = wave_id(), g = lane >> 4, n = lane & 15;
  const u16* asrc;
  if (AF) asrc = act + (size_t)(2 * w) * act_ld * 512 + lane * 8;
  else { const int r = lane >> 3; asrc = act + (size_t)(32 * w + r) * act_ld + (((lane & 7) ^ r) << 3); }
  const u16* wptr[WI];
#pragma unroll
  for (int i = 0; i < WI; ++i) {
    const int k = (w * WI + i) * RPI + lane / CPR, c = (lane % CPR) ^ (wswz(k) << 1);
    wptr[i] = (c < CPR / 2 ? wsrc0 + c * 8 : wsrc1 + (c - CPR / 2) * 8) + (size_t)k * ldw;
  }
  auto issue = [&](int c, int buf) {
    const u32 b = lds + buf * STAGE;
#pragma unroll
    for (int i = 0; i < 4; ++i) {
      if (AF) glds16(asrc + ((size_t)(i >> 1) * act_ld + 2 * c + (i & 1)) * 512, b + (w * 4 + i) * 1024);
      else glds16(asrc + (size_t)(8 * i) * act_ld + c * 64, b + (w * 4 + i) * 1024);
    }
#pragma unroll
    for (int i = 0; i < WI; ++i) glds16(wptr[i] + (size_t)c * 64 * ldw, b + 16384 + (w * WI + i) * 1024);
  };
  u32 xo[2], wo_[2];
#pragma unroll
  for (int kk = 0; kk < 2; ++kk) xo[kk] = AF ? (u32)(kk * 1024 + lane * 16) : (u32)(n * 128 + (((4 * kk + g) ^ (n & 7)) << 4));
#pragma unroll
  for (int hf = 0; hf < 2; ++hf) {
    const int k = (AF ? 16 * hf + 4 * g : 8 * g + 4 * hf) + (n >> 2);
    wo_[hf] = (u32)(16384 + k * RB + w * NPW * 32 + (n & 3) * 8) ^ (u32)(wswz(k) << 5);
  }
  const int nc = K / 64;
  issue(0, 0);
  issue(1, 1);
  int buf = 0;
  for (int c = 0; c < nc; ++c) {
    if (c + 1 < nc) { if (WI == 8) WAIT_VM(12); else WAIT_VM(8); } else WAIT_VM(0);
    BARRIER();
    if (c + 2 < nc) issue(c + 2, buf == 0 ? 2 : buf - 1);
    const u32 b = lds + buf * STAGE;
#pragma unroll
    for (int kk = 0; kk < 2; ++kk) {
      v4u wf[NPW];
#pragma unroll
      for (int t = 0; t < NPW; ++t) {
        const v2u lo = trread(b + ((wo_[0] ^ (u32)(t << 5)) + kk * 32 * RB)), hi = trread(b + ((wo_[1] ^ (u32)(t << 5)) + kk * 32 * RB));
        wf[t] = v4u{lo.x, lo.y, hi.x, hi.y};
      }
#pragma unroll
      for (int m = 0; m < 8; ++m) {
        const v4u xf = *(const LAS v4u*)(b + xo[kk] + m * 2048);
#pragma unroll
        for (int t = 0; t < NPW; ++t) acc[m][t] = WFIRST ? mfma32(wf[t], xf, acc[m][t]) : mfma32(xf, wf[t], acc[m][t]);
      }
    }
    buf = buf == 2 ? 0 : buf + 1;
  }
  WAIT_LGKM0();
  BARRIER();
}

__device__ __forceinline__ float silu(float v) { return v / (1.0f + __expf(-v)); }

__device__ __forceinline__ void x_tile(const Params& p, u32 lds, int rt, int j) {
  const int lane = threadIdx.x & 63, w = wave_id();
  f32x4 acc[8][4];
#pragma unroll
  for (int m = 0; m < 8; ++m)
#pragma unroll
    for (int t = 0; t < 4; ++t) acc[m][t] = f32x4{0.f, 0.f, 0.f, 0.f};
  const u16* xa = p.x + (size_t)rt * 128 * DM;
  const bool vwave = j < 32 && w >= 2;
  const u16 *w0, *w1;
  int ldw;
  if (j < 32) { w0 = p.wk + j * DH; w1 = p.wv + j * DH; ldw = HD; }
  else if (j < 48) { w0 = p.wq + (j - 32) * 256; w1 = w0 + 128; ldw = HD; }
  else { w0 = p.w1 + (j - 48) * 256; w1 = w0 + 128; ldw = FF; }
  if (vwave) gemm_core<4, false, false>(acc, lds, xa, DM, w0, w1, ldw, DM);
  else gemm_core<4, false, true>(acc, lds, xa, DM, w0, w1, ldw, DM);
  if (vwave) {
    v4u* dst = (v4u*)(p.ws + WS_VF);
#pragma unroll
    for (int m2 = 0; m2 < 4; ++m2)
#pragma unroll
      for (int t = 0; t < 4; ++t) {
        const f32x4 a = acc[2 * m2][t], b = acc[2 * m2 + 1][t];
        dst[((size_t)(rt * 4 + m2) * 256 + j * 8 + (w - 2) * 4 + t) * 64 + lane] = v4u{pk2(a[0], a[1]), pk2(a[2], a[3]), pk2(b[0], b[1]), pk2(b[2], b[3])};
      }
  } else {
    v4u* dst; int nks, ks0;
    if (j < 32) { dst = (v4u*)(p.ws + WS_KF); nks = 128; ks0 = 4 * j + 2 * w; }
    else if (j < 48) { dst = (v4u*)(p.ws + WS_QF); nks = 128; ks0 = 8 * (j - 32) + 2 * w; }
    else { dst = (v4u*)(p.ws + WS_HF); nks = 56; ks0 = 8 * (j - 48) + 2 * w; }
    const bool act = j >= 48;
#pragma unroll
    for (int m = 0; m < 8; ++m)
#pragma unroll
      for (int pr = 0; pr < 2; ++pr) {
        f32x4 a = acc[m][2 * pr], b = acc[m][2 * pr + 1];
        if (act) {
#pragma unroll
          for (int r = 0; r < 4; ++r) { a[r] = silu(rbf(a[r])); b[r] = silu(rbf(b[r])); }
        }
        dst[((size_t)(rt * 8 + m) * nks + ks0 + pr) * 64 + lane] = v4u{pk2(a[0], a[1]), pk2(a[2], a[3]), pk2(b[0], b[1]), pk2(b[2], b[3])};
      }
  }
}

__global__ __launch_bounds__(256, 1) void k_gemm_x(Params p) {
  extern __shared__ __attribute__((aligned(16))) unsigned char smem[];
  const int t = blockIdx.x;
  x_tile(p, lds_base(smem), t & 15, t >> 4);
}

constexpr int CH_KX = 131072, CH_FI = CH_KX + 8192, CH_OI = CH_FI + 4096, CH_LDS = CH_OI + 8 * 272;

__device__ __forceinline__ bool nosent(v4u x) { return x.x != SENT && x.y != SENT && x.z != SENT && x.w != SENT; }

__device__ __forceinline__ void load_wslice(v8u (&dst)[2][8], u32 lds, const u16* src) {
  const int lane = threadIdx.x & 63, w = wave_id(), g = lane >> 4, n = lane & 15;
#pragma unroll 4
  for (int i = 0; i < 32; ++i) {
    const int r = (w * 32 + i) * 4 + g;
    const int f = ((r >> 1) & 1) | (((r >> 2) & 1) << 1) | (((r >> 4) & 1) << 2);
    glds16(src + (size_t)r * HD + ((n ^ (f << 1)) << 3), lds + (w * 32 + i) * 1024);
  }
  WAIT_VM(0);
  BARRIER();
  const int qq = n >> 2, pp = n & 3;
#pragma unroll
  for (int q = 0; q < 4; ++q) {
    const int r = 32 * (q >> 1) + 8 * (g >> 1) + 2 * (g & 1) + (q & 1) + 16 * (qq & 1) + 4 * (qq >> 1);
    const int f = ((r >> 1) & 1) | (((r >> 2) & 1) << 1) | (((r >> 4) & 1) << 2);
#pragma unroll
    for (int t = 0; t < 2; ++t) {
      const u32 a = lds + r * 256 + (((2 * w + t) ^ f) << 5) + pp * 8;
#pragma unroll
      for (int ks = 0; ks < 8; ++ks) {
        const v2u v = trread(a + ks * 16384);
        dst[t][ks][2 * q] = v.x;
        dst[t][ks][2 * q + 1] = v.y;
      }
    }
  }
  WAIT_LGKM0();
  BARRIER();
}

__device__ __forceinline__ float max3(float a, float b, float c) { float r; asm("v_max3_f32 %0, %1, %2, %3" : "=v"(r) : "v"(a), "v"(b), "v"(c)); return r; }
__device__ __forceinline__ float max2(float a, float b) { float r; asm("v_max_f32 %0, %1, %2" : "=v"(r) : "v"(a), "v"(b)); return r; }
__device__ __forceinline__ float max_g2(float v) {
  auto r = __builtin_amdgcn_permlane16_swap(__float_as_uint(v), __float_as_uint(v), false, false);
  u32 r0 = r[0], r1 = r[1];
  v = max2(__uint_as_float(r0), __uint_as_float(r1));
  auto q = __builtin_amdgcn_permlane32_swap(__float_as_uint(v), __float_as_uint(v), false, false);
  u32 q0 = q[0], q1 = q[1];
  return max2(__uint_as_float(q0), __uint_as_float(q1));
}
__device__ __forceinline__ u32 sentmin(v4u x) { return min(min(x.x ^ SENT, x.y ^ SENT), min(x.z ^ SENT, x.w ^ SENT)); }
#define SCHED() __builtin_amdgcn_sched_barrier(0)
#define UNI(x) __builtin_amdgcn_readfirstlane(x)
__device__ __forceinline__ v8u ldfrag(u32 a) {
  const v4u lo = *(const LAS v4u*)(a), hi = *(const LAS v4u*)(a + 1024);
  return v8u{lo.x, lo.y, lo.z, lo.w, hi.x, hi.y, hi.z, hi.w};
}

__device__ __forceinline__ void chain_role(const Params& p, u32 lds, int h) {
  const int lane = threadIdx.x & 63, w = wave_id(), g = lane >> 4, n = lane & 15;
  u32* ctrl = (u32*)(p.ws + WS_CTRL);
  const int par = UNI(ctrl[CTRL_PAR]) & 1;
  const __amdgpu_buffer_rsrc_t rw = __builtin_amdgcn_make_buffer_rsrc((void*)(p.ws + WS_WIN), 0, (int)(2 * MB), RSRC_W3);
  const __amdgpu_buffer_rsrc_t rk = __builtin_amdgcn_make_buffer_rsrc((void*)(p.ws + WS_KF), 0, (int)(48 * MB), RSRC_W3);
  const __amdgpu_buffer_rsrc_t rm = __builtin_amdgcn_make_buffer_rsrc((void*)(p.ws + WS_KM), 0, (int)(8 * MB), RSRC_W3);

  v8u wq[2][8], wk[2][8], wv[2][8];
  load_wslice(wq, lds, p.wq + h * DH);
#pragma unroll
  for (int i = 0; i < 16; ++i) asm volatile("" : "+a"(wq[i >> 3][i & 7]));
  load_wslice(wk, lds, p.wk + h * DH);
#pragma unroll
  for (int i = 0; i < 16; ++i) asm volatile("" : "+a"(wk[i >> 3][i & 7]));
  load_wslice(wv, lds, p.wv + h * DH);
#pragma unroll
  for (int i = 0; i < 16; ++i) asm volatile("" : "+v"(wv[i >> 3][i & 7]));
  {
#pragma unroll 4
    for (int i = 0; i < 32; ++i) {
      const int r = w * 32 + i;
      const int f = (r & 1) | (((r >> 2) & 1) << 1) | (((r >> 3) & 1) << 2);
      glds16(p.wo + (size_t)(h * DH + r) * DM + ((lane ^ (f << 1)) << 3), lds + r * 1024);
    }
    WAIT_VM(0);
    BARRIER();
    const int qq = n >> 2, r = 16 * (g >> 1) + 4 * (g & 1) + (qq & 1) + 8 * (qq >> 1);
    const int f = (r & 1) | (((r >> 2) & 1) << 1) | (((r >> 3) & 1) << 2);
    const u32 a0 = lds + r * 1024 + w * 256 + (f << 5) + (n & 3) * 8;
#pragma unroll
    for (int ks = 0; ks < 2; ++ks) {
      v2u tmp[8][4];
#pragma unroll
      for (int t = 0; t < 8; ++t)
#pragma unroll
        for (int q = 0; q < 4; ++q) tmp[t][q] = trread((a0 ^ (u32)(t << 5)) + ks * 65536 + (q >> 1) * 32768 + (q & 1) * 2048);
      WAIT_LGKM0();
      BARRIER();
#pragma unroll
      for (int t = 0; t < 8; ++t)
#pragma unroll
        for (int hf = 0; hf < 2; ++hf)
          *(LAS v4u*)(lds + ks * 65536 + w * 16384 + t * 2048 + hf * 1024 + lane * 16) = v4u{tmp[t][2 * hf].x, tmp[t][2 * hf].y, tmp[t][2 * hf + 1].x, tmp[t][2 * hf + 1].y};
    }
  }
  const int kofs = ((w >> 1) * 128 + 4 * h + 2 * (w & 1)) * 1024 + lane * 16;
  const int vofs = (int)(32 * MB) + (8 * h + 2 * w) * 1024 + lane * 16;
  const u32 kx_w = lds + CH_KX + 2 * w * 1024 + lane * 16, kx_r = lds + CH_KX + lane * 16;
  v4u kn[2], vc[2];
#pragma unroll
  for (int i = 0; i < 2; ++i) {
    kn[i] = __builtin_amdgcn_raw_buffer_load_b128(rk, kofs + i * 1024, 0, SC1);
    vc[i] = __builtin_amdgcn_raw_buffer_load_b128(rk, vofs + i * 1024, 0, SC1);
  }
#pragma unroll
  for (int i = 0; i < 2; ++i) *(LAS v4u*)(kx_w + i * 1024) = kn[i];
  WAIT_VM(0);
  WAIT_LGKM0();
  BARRIER();

  const int idx = (n & 8) ? 0xEEEE : 0x4444;
  const int soff = remap(WIN_PART + ((((2 * w) * 4 + 2 * (g >> 1) + (n >> 3)) * 2 + ((n >> 2) & 1)) * 2 + (g & 1)) * 2048 + h * 64 + (n & 3) * 16, par);
  const int loff = remap(WIN_PART + (4 * h + w) * 2048 + lane * 16, par);
  const int moff = remap(WIN_MEM + ((lane >> 2) & 3) * 8192 + (4 * h + w) * 64 + g * 16 + (lane & 3) * 4, par);
  const int goff = remap(WIN_MEM + (h & 3) * 8192 + lane * 16, par);
  const int kmoff = n >= 8 ? h * 2048 + (w * 4 + g) * 128 + (n & 7) * 16 : 0x40000000  , vmoff = (int)(4 * MB) + h * 2048 + w * 512 + lane * 8;
  const u32 fi_w = lds + CH_FI + (32 * w + 16 * (g >> 1) + n) * 32 + (g & 1) * 8;
  const u32 fi_r = lds + CH_FI + (4 * g + (n >> 2)) * 32 + (n & 3) * 8;
  const u32 oi_w = lds + CH_OI + (n & 7) * 272 + (32 * w + 4 * g) * 2;
  const u32 oi_r = lds + CH_OI + (n & 7) * 272 + (16 * g + 8 * (n >> 3)) * 2;
  const u32 wo_r = lds + w * 16384 + lane * 16;
  const v4u sent4 = {SENT, SENT, SENT, SENT};
  int mo = 0;
  u32 fail = 0;

  for (int s = 0;; ++s) {
    f32x4 aq[2], ak[2], av[2];
    v4u xa[8];
    for (u32 spins = 0;;) {
      asm volatile("" ::: "memory");
#pragma unroll
      for (int ks = 0; ks < 8; ++ks) xa[ks] = __builtin_amdgcn_raw_buffer_load_b128(rw, goff, mo + ks * 1024, SC1);
#pragma unroll
      for (int t = 0; t < 2; ++t) { aq[t] = vzero(); ak[t] = vzero(); }
      u32 mn = ~0u;
      SCHED();
#pragma unroll
      for (int ks = 0; ks < 8; ++ks) {
#pragma unroll
        for (int t = 0; t < 2; ++t) { aq[t] = smf(xa[ks], wq[t][ks], aq[t], idx); ak[t] = smf(xa[ks], wk[t][ks], ak[t], idx); }
        mn = min(mn, sentmin(xa[ks]));
        SCHED();
      }
      if (__all(mn != 0u)) break;
      if (++spins > SPIN_CAP) { fail = 2; break; }
    }
    if (fail) break;
    av[0] = vzero(); av[1] = vzero();
#pragma unroll
    for (int ks = 0; ks < 2; ++ks)
#pragma unroll
      for (int t = 0; t < 2; ++t) av[t] = smf(xa[ks], wv[t][ks], av[t], idx);
    SCHED();
    {
      float q4[4], k4[4];
#pragma unroll
      for (int r = 0; r < 4; ++r) { q4[r] = fold32(aq[0][r], aq[1][r]); k4[r] = fold32(ak[0][r], ak[1][r]); }
      *(LAS v2u*)(fi_w) = v2u{pk2(q4[0], q4[1]), pk2(q4[2], q4[3])};
      *(LAS v2u*)(fi_w + 16) = v2u{pk2(k4[0], k4[1]), pk2(k4[2], k4[3])};
    }
    SCHED();
#pragma unroll
    for (int ks = 2; ks < 4; ++ks)
#pragma unroll
      for (int t = 0; t < 2; ++t) av[t] = smf(xa[ks], wv[t][ks], av[t], idx);
    WAIT_LGKM0();
    BARRIER();
    v4u F[4], k0[4], k1[4];
#pragma unroll
    for (int ks = 0; ks < 4; ++ks) {
      const v2u lo = trread(fi_r + ks * 1024), hi = trread(fi_r + ks * 1024 + 512);
      F[ks] = v4u{lo.x, lo.y, hi.x, hi.y};
      k0[ks] = *(const LAS v4u*)(kx_r + ks * 1024);
      k1[ks] = *(const LAS v4u*)(kx_r + 4096 + ks * 1024);
    }
    SCHED();
#pragma unroll
    for (int ks = 4; ks < 8; ++ks)
#pragma unroll
      for (int t = 0; t < 2; ++t) av[t] = smf(xa[ks], wv[t][ks], av[t], idx);
    SCHED();
    f32x4 t0, t1, t2;
#pragma unroll
    for (int ks = 0; ks < 4; ++ks) {
      if (ks == 0) { mfma32_v0(t0, F[ks], F[ks]); mfma32_v0(t1, k0[ks], F[ks]); mfma32_v0(t2, k1[ks], F[ks]); }
      else { mfma32_va(t0, F[ks], F[ks]); mfma32_va(t1, k0[ks], F[ks]); mfma32_va(t2, k1[ks], F[ks]); }
    }
    SCHED();
    v2u vm;
    {
      float v4[4];
#pragma unroll
      for (int r = 0; r < 4; ++r) v4[r] = fold32(av[0][r], av[1][r]);
      vm = v2u{pk2(v4[0], v4[1]), pk2(v4[2], v4[3])};
    }
    {
      const v4u fa = (w & 1) ? F[1] : F[0], fb = (w & 1) ? F[3] : F[2], fk = (w & 2) ? fb : fa;
      __builtin_amdgcn_raw_buffer_store_b128(fk, rm, kmoff, s * 65536, SC1);
      __builtin_amdgcn_raw_buffer_store_b64(vm, rm, vmoff, s * 65536, SC1);
    }
    if (s == NSEG - 1) break;
    const int ps = (s & 1) * 524288;
    if (s > 0) {
      __builtin_amdgcn_raw_buffer_store_b128(sent4, rw, loff, ps ^ 524288, SC1);
      __builtin_amdgcn_raw_buffer_store_b128(sent4, rw, loff, (ps ^ 524288) + 1024, SC1);
      __builtin_amdgcn_raw_buffer_store_b32(SENT, rw, moff, mo == 0 ? 131072 : mo - 65536, SC1);
    }
#pragma unroll
    for (int i = 0; i < 2; ++i) kn[i] = __builtin_amdgcn_raw_buffer_load_b128(rk, kofs, (s + 1) * 262144 + i * 1024, SC1);
    v8u wb[6];
#pragma unroll
    for (int i = 0; i < 6; ++i) wb[i] = ldfrag(wo_r + (i & 1) * 65536 + (i >> 1) * 2048);
    SCHED();
    asm volatile("s_nop 0" : "+v"(t0), "+v"(t1), "+v"(t2));
    if (lane < 32) t0 = t1;
    float mx = max3(max3(t0[0], t0[1], t0[2]), max3(t0[3], t1[0], t1[1]), max3(max3(t1[2], t1[3], t2[0]), max3(t2[1], t2[2], t2[3]), t2[3]));
    mx = max_g2(mx);
    const float mc = mx * SCALE_LOG2E;
#pragma unroll
    for (int r = 0; r < 4; ++r) {
      t1[r] = __builtin_amdgcn_exp2f(t1[r] * SCALE_LOG2E - mc);
      t2[r] = __builtin_amdgcn_exp2f(t2[r] * SCALE_LOG2E - mc);
    }
    v4u pt = {pk2(t1[0], t1[1]), pk2(t1[2], t1[3]), pk2(t2[0], t2[1]), pk2(t2[2], t2[3])};
    f32x4 o0, o1;
    asm volatile("s_nop 1" : "+v"(pt), "+v"(vc[0]), "+v"(vc[1]));
    mfma32_v0(o0, vc[0], pt);
    mfma32_v0(o1, vc[1], pt);
    asm volatile("" : "+v"(t0));
#pragma unroll
    for (int r = 0; r < 4; ++r) t0[r] = lane < 32 ? 0.f : __builtin_amdgcn_exp2f(t0[r] * SCALE_LOG2E - mc);
    v2u pmh = {pk2(t0[0], t0[1]), pk2(t0[2], t0[3])};
    v2u pml;
    {
      auto r0 = __builtin_amdgcn_permlane32_swap(pmh.x, 0u, false, false);
      auto r1 = __builtin_amdgcn_permlane32_swap(pmh.y, 0u, false, false);
      u32 a = r0[1], b = r1[1];
      pml = v2u{a, b};
    }
    asm volatile("s_nop 1" : "+v"(pml), "+v"(pmh), "+v"(vm));
    mfma16_va(o0, vm, pml);
    mfma16_va(o1, vm, pmh);
    const float ls = ((t0[0] + t0[1]) + (t0[2] + t0[3])) + ((t1[0] + t1[1]) + (t1[2] + t1[3])) + ((t2[0] + t2[1]) + (t2[2] + t2[3]));
    float inv = __builtin_amdgcn_rcpf(sum_g(ls));
    asm volatile("s_nop 7" : "+v"(o0), "+v"(o1), "+v"(inv));
    if (n < 8) {
      *(LAS v2u*)(oi_w) = v2u{pk2(o0[0] * inv, o0[1] * inv), pk2(o0[2] * inv, o0[3] * inv)};
      *(LAS v2u*)(oi_w + 32) = v2u{pk2(o1[0] * inv, o1[1] * inv), pk2(o1[2] * inv, o1[3] * inv)};
    }
    f32x4 c[8];
#pragma unroll
    for (int t = 0; t < 4; ++t) c[t] = vzero();
#pragma unroll
    for (int i = 0; i < 2; ++i) vc[i] = __builtin_amdgcn_raw_buffer_load_b128(rk, vofs, (s + 1) * 262144 + i * 1024, SC1);
    WAIT_LGKM0();
    BARRIER();
    const v4u oa0 = *(const LAS v4u*)(oi_r), oa1 = *(const LAS v4u*)(oi_r + 128);
    SCHED();
#pragma unroll
    for (int i = 0; i < 16; ++i) {
      if (i >= 6 && i < 14 && !(i & 1)) c[(i >> 1) + 1] = vzero();
      c[i >> 1] = smf((i & 1) ? oa1 : oa0, wb[i % 6], c[i >> 1], idx);
      if (i + 6 < 16) wb[i % 6] = ldfrag(wo_r + (i & 1) * 65536 + ((i + 6) >> 1) * 2048);
      if (i == 9 || i == 15) {
        const int i2 = i == 15, cb = 4 * i2;
        if (i == 15) SCHED();
        float x0[4], x1[4];
#pragma unroll
        for (int r = 0; r < 4; ++r) { x0[r] = fold32(c[cb][r], c[cb + 2][r]); x1[r] = fold32(c[cb + 1][r], c[cb + 3][r]); }
        __builtin_amdgcn_raw_buffer_store_b128(v4u{pk2(x0[0], x0[1]), pk2(x0[2], x0[3]), pk2(x1[0], x1[1]), pk2(x1[2], x1[3])}, rw, soff, ps + i2 * 65536, SC1);
      }
      SCHED();
    }
#pragma unroll
    for (int i = 0; i < 2; ++i) *(LAS v4u*)(kx_w + i * 1024) = kn[i];
    WAIT_VM(0);
    mo = mo == 131072 ? 0 : mo + 65536;
    {
      v4u y0, y1;
      for (u32 spins = 0;;) {
        asm volatile("" ::: "memory");
        y0 = __builtin_amdgcn_raw_buffer_load_b128(rw, loff, ps, SC1);
        y1 = __builtin_amdgcn_raw_buffer_load_b128(rw, loff, ps + 1024, SC1);
        if (__all(min(sentmin(y0), sentmin(y1)) != 0u)) break;
        if (++spins > SPIN_CAP) { fail = 1; break; }
      }
      if (fail) break;
      float a[8];
#pragma unroll
      for (int d = 0; d < 4; ++d) { a[2 * d] = bflo(y0[d]) + bflo(y1[d]); a[2 * d + 1] = bfhi(y0[d]) + bfhi(y1[d]); }
      const float b00 = fold32(a[0], a[2]), b01 = fold32(a[1], a[3]), b10 = fold32(a[4], a[6]), b11 = fold32(a[5], a[7]);
      float c0 = fold16(b00, b01), c1 = fold16(b10, b11);
      c0 += ror8(c0); c1 += ror8(c1);
      c0 += ror4(c0); c1 += ror4(c1);
      __builtin_amdgcn_raw_buffer_store_b32(pk2(c0, c1), rw, moff, mo, SC1);
    }
    WAIT_VM(0);
  }
  if (fail && lane == 0) __hip_atomic_store(ctrl + CTRL_TMO, fail, __ATOMIC_RELAXED, __HIP_MEMORY_SCOPE_AGENT);
}

__global__ __launch_bounds__(256, 1) void k_chain(Params p) {
  extern __shared__ __attribute__((aligned(16))) unsigned char smem[];
  const int b = blockIdx.x;
  if ((b & 7) >= 4) return;
  chain_role(p, lds_base(smem), (b >> 3) * 4 + (b & 7));
}

__device__ __forceinline__ void attn_item(const Params& p, int s, int h) {
  const int lane = threadIdx.x & 63, g = lane >> 4, n = lane & 15;
  const v4u* KF = (const v4u*)(p.ws + WS_KF) + ((size_t)(2 * s) * 128 + 4 * h) * 64 + lane;
  const v4u* QF = (const v4u*)(p.ws + WS_QF) + ((size_t)(2 * s) * 128 + 4 * h) * 64 + lane;
  const v4u* VF = (const v4u*)(p.ws + WS_VF) + ((size_t)s * 256 + 8 * h) * 64 + lane;
  const v4u* KM = (const v4u*)(p.ws + WS_KM + (size_t)(s * 32 + h) * 2048) + g * 8 + (n & 7);
  const v2u* VM = (const v2u*)(p.ws + WS_VM + (size_t)(s * 32 + h) * 2048) + lane;
  v4u* OF = (v4u*)(p.ws + WS_OF) + ((size_t)(2 * s) * 128 + 4 * h) * 64 + lane;
  f32x4 t[2][3];
#pragma unroll
  for (int qt = 0; qt < 2; ++qt)
#pragma unroll
    for (int k = 0; k < 3; ++k) t[qt][k] = f32x4{0.f, 0.f, 0.f, 0.f};
#pragma unroll
  for (int ks = 0; ks < 4; ++ks) {
    const v4u k0 = KF[ks * 64], k1 = KF[(128 + ks) * 64], q0 = QF[ks * 64], q1 = QF[(128 + ks) * 64], km = KM[ks * 32];
    t[0][0] = mfma32(km, q0, t[0][0]);
    t[0][1] = mfma32(k0, q0, t[0][1]);
    t[1][0] = mfma32(km, q1, t[1][0]);
    t[1][1] = mfma32(k0, q1, t[1][1]);
    t[1][2] = mfma32(k1, q1, t[1][2]);
  }
  v4u pt[2]; v2u pl[2], ph[2]; float inv[2];
#pragma unroll
  for (int qt = 0; qt < 2; ++qt) {
    float v[12];
#pragma unroll
    for (int r = 0; r < 4; ++r) {
      const int key = 4 * g + r;
      v[r] = t[qt][0][r] * SCALE_LOG2E;
      v[4 + r] = (qt == 1 || key <= n) ? t[qt][1][r] * SCALE_LOG2E : MASKED * 1.4426950408889634f;
      v[8 + r] = (qt == 1 && key <= n) ? t[qt][2][r] * SCALE_LOG2E : MASKED * 1.4426950408889634f;
      if (lane >= 32) v[r] = v[4 + r];
    }
    float mx = v[0];
#pragma unroll
    for (int i = 1; i < 12; ++i) mx = fmaxf(mx, v[i]);
    mx = max_g(mx);
    float ls = 0.f;
#pragma unroll
    for (int i = 0; i < 12; ++i) {
      v[i] = __builtin_amdgcn_exp2f(v[i] - mx);
      if (i < 4 && lane >= 32) v[i] = 0.f;
      ls += v[i];
    }
    inv[qt] = 1.0f / sum_g(ls);
    pt[qt] = v4u{pk2(v[4], v[5]), pk2(v[6], v[7]), pk2(v[8], v[9]), pk2(v[10], v[11])};
    pl[qt] = v2u{pk2(v[0], v[1]), pk2(v[2], v[3])};
    auto r0 = __builtin_amdgcn_permlane32_swap(0u, pl[qt].x, false, false);
    auto r1 = __builtin_amdgcn_permlane32_swap(0u, pl[qt].y, false, false);
    u32 a = r0[0], b = r1[0];
    ph[qt] = v2u{a, b};
  }
#pragma unroll
  for (int e2 = 0; e2 < 4; ++e2) {
    const v4u v0 = VF[(2 * e2) * 64], v1 = VF[(2 * e2 + 1) * 64];
    const v2u vm = VM[e2 * 64];
    const v4u vm4 = {vm.x, vm.y, 0u, 0u};
#pragma unroll
    for (int qt = 0; qt < 2; ++qt) {
      f32x4 a = {0.f, 0.f, 0.f, 0.f}, b = a;
      a = mfma32(v0, pt[qt], a); a = mfma32(vm4, v4u{pl[qt].x, pl[qt].y, 0u, 0u}, a);
      b = mfma32(v1, pt[qt], b); b = mfma32(vm4, v4u{ph[qt].x, ph[qt].y, 0u, 0u}, b);
      const float iv = inv[qt];
      OF[(qt * 128 + e2) * 64] = v4u{pk2(a[0] * iv, a[1] * iv), pk2(a[2] * iv, a[3] * iv), pk2(b[0] * iv, b[1] * iv), pk2(b[2] * iv, b[3] * iv)};
    }
  }
}

__global__ __launch_bounds__(256) void k_attn(Params p) {
  const int item = blockIdx.x * 4 + wave_id();
  attn_item(p, item >> 5, item & 31);
}

__global__ __launch_bounds__(256, 1) void k_out(Params p) {
  extern __shared__ __attribute__((aligned(16))) unsigned char smem[];
  const u32 lds = lds_base(smem);
  const int rt = blockIdx.x & 15, nt = blockIdx.x >> 4;
  const int lane = threadIdx.x & 63, w = wave_id(), g = lane >> 4, n = lane & 15;
  LAS float* gl = (LAS float*)(lds + 3 * 32768);
  {
    const int tok = threadIdx.x >> 1, half = threadIdx.x & 1;
    const v4u* xr = (const v4u*)(p.x + (size_t)(rt * 128 + tok) * DM + half * 256);
    const v4u* gr = (const v4u*)(p.gw + half * 256);
    float sacc = 0.f;
    for (int i = 0; i < 32; ++i) {
      const v4u a = xr[i], b = gr[i];
#pragma unroll
      for (int d = 0; d < 4; ++d) sacc += rbf(bflo(a[d]) * bflo(b[d])) + rbf(bfhi(a[d]) * bfhi(b[d]));
    }
    sacc += __shfl_xor(sacc, 1);
    if (half == 0) gl[tok] = rbf(1.0f / (1.0f + __expf(-rbf(sacc))));
  }
  f32x4 accb[8][2], acca[8][2];
#pragma unroll
  for (int m = 0; m < 8; ++m)
#pragma unroll
    for (int t = 0; t < 2; ++t) { accb[m][t] = f32x4{0.f, 0.f, 0.f, 0.f}; acca[m][t] = accb[m][t]; }
  gemm_core<2, true, true>(accb, lds, (const u16*)(p.ws + WS_HF) + (size_t)rt * 8 * 56 * 512, 56, p.w2 + nt * 128, p.w2 + nt * 128 + 64, DM, FF);
  gemm_core<2, true, true>(acca, lds, (const u16*)(p.ws + WS_OF) + (size_t)rt * 8 * 128 * 512, 128, p.wo + nt * 128, p.wo + nt * 128 + 64, DM, HD);
#pragma unroll
  for (int m = 0; m < 8; ++m) {
    const float gt = gl[16 * m + n], og = rbf(1.0f - gt);
#pragma unroll
    for (int t = 0; t < 2; ++t) {
      float r[4];
#pragma unroll
      for (int i = 0; i < 4; ++i) r[i] = rbf(gt * rbf(acca[m][t][i])) + rbf(og * rbf(accb[m][t][i]));
      *(v2u*)(p.out + (size_t)(rt * 128 + 16 * m + n) * DM + nt * 128 + w * 32 + t * 16 + 4 * g) = v2u{pk2(r[0], r[1]), pk2(r[2], r[3])};
    }
  }
}

extern "C" void kernel_launch(void* const* d_in, const int* in_sizes, int n_in, void* d_out, int out_size, void* d_ws, size_t ws_size,
                              hipStream_t stream) {
  Params p;
  p.x = (const u16*)d_in[0];
  p.mem0 = (const u16*)d_in[1];
  p.wq = (const u16*)d_in[2];
  p.wk = (const u16*)d_in[3];
  p.wv = (const u16*)d_in[4];
  p.wo = (const u16*)d_in[5];
  p.w1 = (const u16*)d_in[8];
  p.w2 = (const u16*)d_in[9];
  p.gw = (const u16*)d_in[10];
  p.out = (u16*)d_out;
  p.ws = (unsigned char*)(((uintptr_t)d_ws + 2 * MB - 1) & ~(uintptr_t)(2 * MB - 1));
  constexpr int LDS_X = 3 * 49152, LDS_O = 3 * 32768 + 512;
  (void)hipFuncSetAttribute((const void*)k_gemm_x, hipFuncAttributeMaxDynamicSharedMemorySize, LDS_X);
  (void)hipFuncSetAttribute((const void*)k_chain, hipFuncAttributeMaxDynamicSharedMemorySize, CH_LDS);
  (void)hipFuncSetAttribute((const void*)k_out, hipFuncAttributeMaxDynamicSharedMemorySize, LDS_O);
  k_init<<<256, 256, 0, stream>>>(p);
  k_gemm_x<<<16 * 55, 256, LDS_X, stream>>>(p);
  k_chain<<<64, 256, CH_LDS, stream>>>(p);
  k_attn<<<NSEG * NH / 4, 256, 0, stream>>>(p);
  k_out<<<64, 256, LDS_O, stream>>>(p);
}
```

```cpp
#include <hip/hip_runtime.h>
#include <hip/hip_bf16.h>
#include <stdint.h>
#include <type_traits>

#define GAS __attribute__((address_space(1)))
#define LAS __attribute__((address_space(3)))
typedef unsigned short u16;
typedef unsigned int u32;
typedef u32 v4u __attribute__((ext_vector_type(4)));
typedef u32 v2u __attribute__((ext_vector_type(2)));
typedef u32 v8u __attribute__((ext_vector_type(8)));
typedef float f32x4 __attribute__((ext_vector_type(4)));
typedef float f32x2 __attribute__((ext_vector_type(2)));
typedef short s16x4 __attribute__((ext_vector_type(4)));
typedef __bf16 bf16x2 __attribute__((ext_vector_type(2)));
typedef __bf16 bf16x8 __attribute__((ext_vector_type(8)));
typedef __bf16 bf16x16 __attribute__((ext_vector_type(16)));

constexpr int SEQ = 2048, DM = 512, NH = 32, DH = 128, NSEG = 64, FF = 1792, HD = NH * DH;
constexpr u32 SENT = 0x7FFFFFFFu;
constexpr int RSRC_W3 = 0x00020000;
constexpr int SC1 = 16;
constexpr float SCALE_LOG2E = 0.08838834764831845f * 1.4426950408889634f;
constexpr float MASKED = -999292928.0f;
constexpr u32 SPIN_CAP = 1u << 17;

constexpr size_t MB = 1024 * 1024;
constexpr size_t WS_WIN = 0;
constexpr size_t WS_CTRL = 2 * MB;
constexpr size_t WS_AB = 3 * MB;
constexpr int AB_K = 0, AB_V = 8192, AB_Q = 16384, AB_M = 24576, AB_BLK = 32768;
constexpr size_t WS_OF = WS_AB + 64 * MB;
constexpr size_t WS_HF = WS_OF + 16 * MB;
constexpr size_t WS_SL = WS_HF + 7 * MB;
constexpr size_t WS_G = WS_SL + 16 * MB;
constexpr size_t WS_END = WS_G + 8192;
constexpr int RD_OF = (int)(WS_OF - WS_AB), RD_HF = (int)(WS_HF - WS_AB), RD_SL = (int)(WS_SL - WS_AB), RD_G = (int)(WS_G - WS_AB), RD_SIZE = (int)(WS_END - WS_AB);
constexpr int WIN_PART = 0;
constexpr int WIN_MEM = 512 * 1024;
constexpr u32 TAG2 = 0x9E3779B9u;
constexpr int CT_EPOCH = 0, CT_PAR = 128  ,  CT_TMO = 256, CT_INIT = 512  , CT_G = 768  , CT_ARR = 1024  ,
              CT_KV = 4096  , CT_OFF = 10240  , CT_H = 12288  ,
              CT_O = 16384  , CT_T = 32768  , CT_PROG = 49152  , CT_BYTES = 114688;
constexpr int CTRL_PAR = CT_PAR / 4 + 1, CTRL_TMO = CT_TMO / 4;

struct Params {
  const u16 *x, *mem0, *wq, *wk, *wv, *wo, *w1, *w2, *gw;
  u16* out;
  unsigned char* ws;
};

__device__ __forceinline__ u32 pk2(float a, float b) {
  f32x2 v = {a, b};
  bf16x2 r = __builtin_convertvector(v, bf16x2);
  return __builtin_bit_cast(u32, r);
}
__device__ __forceinline__ float bflo(u32 u) { return __uint_as_float(u << 16); }
__device__ __forceinline__ float bfhi(u32 u) { return __uint_as_float(u & 0xFFFF0000u); }
__device__ __forceinline__ float rbf(float a) { return bflo(pk2(a, a) & 0xFFFFu); }
__device__ __forceinline__ f32x4 mfma32(v4u a, v4u b, f32x4 c) {
  return __builtin_amdgcn_mfma_f32_16x16x32_bf16(__builtin_bit_cast(bf16x8, a), __builtin_bit_cast(bf16x8, b), c, 0, 0, 0);
}
__device__ __forceinline__ f32x4 mfma16(v2u a, v2u b, f32x4 c) {
  return __builtin_amdgcn_mfma_f32_16x16x16bf16_1k(__builtin_bit_cast(s16x4, a), __builtin_bit_cast(s16x4, b), c, 0, 0, 0);
}
__device__ __forceinline__ f32x4 smf(v4u a, v8u b, f32x4 c, int idx) {
  return __builtin_amdgcn_smfmac_f32_16x16x64_bf16(__builtin_bit_cast(bf16x8, a), __builtin_bit_cast(bf16x16, b), c, idx, 0, 0);
}
__device__ __forceinline__ f32x4 vzero() {
  f32x4 z = {0.f, 0.f, 0.f, 0.f};
  asm volatile("" : "+v"(z));
  return z;
}
__device__ __forceinline__ void mfma32_v0(f32x4& d, v4u a, v4u b) { asm volatile("v_mfma_f32_16x16x32_bf16 %0, %1, %2, 0" : "=&v"(d) : "v"(a), "v"(b)); }
__device__ __forceinline__ void mfma32_va(f32x4& d, v4u a, v4u b) { asm volatile("v_mfma_f32_16x16x32_bf16 %0, %1, %2, %0" : "+v"(d) : "v"(a), "v"(b)); }
__device__ __forceinline__ void mfma16_va(f32x4& d, v2u a, v2u b) { asm volatile("v_mfma_f32_16x16x16_bf16 %0, %1, %2, %0" : "+v"(d) : "v"(a), "v"(b)); }
__device__ __forceinline__ v2u trread(u32 lds_addr) {
  s16x4 v = __builtin_amdgcn_ds_read_tr16_b64_v4i16((LAS s16x4*)lds_addr);
  return __builtin_bit_cast(v2u, v);
}
__device__ __forceinline__ float fold32(float a, float b) {
  auto r = __builtin_amdgcn_permlane32_swap(__float_as_uint(a), __float_as_uint(b), false, false);
  u32 r0 = r[0], r1 = r[1];
  return __uint_as_float(r0) + __uint_as_float(r1);
}
__device__ __forceinline__ float fold16(float a, float b) {
  auto r = __builtin_amdgcn_permlane16_swap(__float_as_uint(a), __float_as_uint(b), false, false);
  u32 r0 = r[0], r1 = r[1];
  return __uint_as_float(r0) + __uint_as_float(r1);
}
__device__ __forceinline__ float max_g(float v) {
  auto r = __builtin_amdgcn_permlane16_swap(__float_as_uint(v), __float_as_uint(v), false, false);
  u32 r0 = r[0], r1 = r[1];
  v = fmaxf(__uint_as_float(r0), __uint_as_float(r1));
  auto q = __builtin_amdgcn_permlane32_swap(__float_as_uint(v), __float_as_uint(v), false, false);
  u32 q0 = q[0], q1 = q[1];
  return fmaxf(__uint_as_float(q0), __uint_as_float(q1));
}
__device__ __forceinline__ float sum_g(float v) { v = fold16(v, v); return fold32(v, v); }
__device__ __forceinline__ float ror4(float v) { return __int_as_float(__builtin_amdgcn_update_dpp(0, __float_as_int(v), 0x124, 0xf, 0xf, true)); }
__device__ __forceinline__ float ror8(float v) { return __int_as_float(__builtin_amdgcn_update_dpp(0, __float_as_int(v), 0x128, 0xf, 0xf, true)); }
template <int OFF>
__device__ __forceinline__ void glds16o(const void* gsrc, u32 base) {
  u32 keep;
  asm volatile("s_mov_b32 %0, m0\n\ts_add_u32 m0, %2, %3\n\ts_nop 0\n\tglobal_load_lds_dwordx4 %1, off\n\ts_mov_b32 m0, %0"
               : "=&s"(keep) : "v"(gsrc), "s"(base), "i"(OFF) : "memory", "scc");
}
__device__ __forceinline__ void glds16(const void* gsrc, u32 lds_dst) { glds16o<0>(gsrc, lds_dst); }
template <int OFF, int X, int S>
__device__ __forceinline__ void blds16(__amdgpu_buffer_rsrc_t rs, u32 voff, u32 soff, u32 base) {
  u32 vt, st;
  asm volatile("s_add_u32 m0, %5, %6\n\tv_xor_b32 %0, %7, %3\n\ts_add_u32 %1, %4, %8\n\tbuffer_load_dwordx4 %0, %2, %1 offen lds"
               : "=&v"(vt), "=&s"(st) : "s"(rs), "v"(voff), "s"(soff), "s"(base), "i"(OFF), "i"(X), "i"(S) : "memory", "scc");
}
__device__ __forceinline__ u32 m0_save() { u32 k; asm volatile("s_mov_b32 %0, m0" : "=s"(k)); return k; }
__device__ __forceinline__ void m0_restore(u32 k) { asm volatile("s_mov_b32 m0, %0" :: "s"(k)); }
__device__ __forceinline__ __amdgpu_buffer_rsrc_t srd(const void* base, size_t bytes) { return __builtin_amdgcn_make_buffer_rsrc((void*)base, 0, (int)bytes, RSRC_W3); }
template <int N, class F> __device__ __forceinline__ void static_for(F&& f) {
  if constexpr (N > 0) { static_for<N - 1>(f); f(std::integral_constant<int, N - 1>{}); }
}
#define WAIT_VM(n) asm volatile("s_waitcnt vmcnt(" #n ")" ::: "memory")
#define WAIT_LGKM0() asm volatile("s_waitcnt lgkmcnt(0)" ::: "memory")
#define BARRIER() __builtin_amdgcn_s_barrier()
__device__ __forceinline__ int lane_id() { int l; asm volatile("v_mbcnt_lo_u32_b32 %0, -1, 0\n\tv_mbcnt_hi_u32_b32 %0, -1, %0" : "=v"(l)); return l; }
struct Ctx : Params { int w, b, lab; u32 tag; };
__device__ __forceinline__ u32 lds_base(unsigned char* smem) { return (u32)(uintptr_t)(LAS unsigned char*)smem; }
__device__ __forceinline__ int remap(int a, int par) { return ((a >> 13) << 14) + (par << 13) + (a & 8191); }

#define TLS(i)
typedef GAS u32 gu32;
#define RLX_AGENT __ATOMIC_RELAXED, __HIP_MEMORY_SCOPE_AGENT
__device__ __forceinline__ gu32* ctw(const Ctx& p, int off) { return (gu32*)(p.ws + WS_CTRL + off); }
typedef GAS unsigned long long gu64;
__device__ __forceinline__ u32 epoch_ask(const Ctx& p) {
  u32 ev;
  asm volatile("global_load_dword %0, %1, %2 sc1" : "=v"(ev) : "v"(0), "s"(p.ws + WS_CTRL + CT_EPOCH) : "memory");
  return ev;
}
#define TELL_XCD(ev, N) do { if (p.b == 0 && p.w == 0) { WAIT_VM(N); tell_xcd(p, ev); } } while (0)
__device__ __forceinline__ void tell_xcd(const Ctx& p, u32 ev) {
  asm volatile("" : "+v"(ev));
  const u32 t = ev + 1u;
  __hip_atomic_store((gu64*)ctw(p, CT_OFF + (lane_id() & 15) * 128), (unsigned long long)((t ^ TAG2) + (p.lab & 7)) << 32 | t, RLX_AGENT);
}
__device__ __forceinline__ void restart(const Ctx& p, u32 lab  ) {
  asm volatile("s_waitcnt vmcnt(0) lgkmcnt(0)\n\ts_barrier\n\ts_getpc_b64 s[4:5]\n"
               "1:\n\ts_sub_u32 s4, s4, 1b-_Z6k_main6Params\n\ts_subb_u32 s5, s5, 0\n\ts_setpc_b64 s[4:5]"
               :: "{s[0:1]}"(__builtin_amdgcn_kernarg_segment_ptr()), "{s2}"(p.b | (lab + 8) << 16), "{v0}"(p.w * 64 + lane_id()) : "memory", "s4", "s5", "scc");
  __builtin_unreachable();
}
__device__ __forceinline__ void adopt_tag(Ctx& p, u32 ev, bool first = true) {
  asm volatile("" : "+v"(ev));
  if (!first) return;
  p.tag = __builtin_amdgcn_readfirstlane(ev) + 1u;
  if (p.lab < 8) {
    u32 lab = p.lab;
    for (u32 spins = 0; spins < SPIN_CAP; ++spins) {
      const unsigned long long v = __hip_atomic_load((gu64*)ctw(p, CT_OFF + (p.b & 15) * 128), RLX_AGENT);
      const u32 lo = __builtin_amdgcn_readfirstlane((u32)v), d = __builtin_amdgcn_readfirstlane((u32)(v >> 32)) - (p.tag ^ TAG2);
      if (lo == p.tag && d < 8u) { lab = (p.b + d) & 7; break; }
      __builtin_amdgcn_s_sleep(1);
    }
    if (lab != (u32)p.lab) restart(p, lab);
  }
  if (p.w == 0 && lane_id() == 0) __hip_atomic_store(ctw(p, CT_ARR + p.b * 4), p.tag, RLX_AGENT);
}
__device__ __forceinline__ unsigned long long tag64(const Ctx& p) { return (unsigned long long)(p.tag ^ TAG2) << 32 | p.tag; }
template <bool WIDE, int SLEEP = 1>
__device__ __forceinline__ bool wait_set(const Ctx& p, int off, int n) {
  const int l = min(lane_id(), n - 1);
  for (u32 spins = 0;; ++spins) {
    const bool ok = WIDE ? __hip_atomic_load((gu64*)ctw(p, off) + l, RLX_AGENT) == tag64(p) : __hip_atomic_load(ctw(p, off) + l, RLX_AGENT) == p.tag;
    if (__all(ok)) return true;
    if (spins > SPIN_CAP || ((spins & 1023u) == 1023u && __builtin_amdgcn_readfirstlane(__hip_atomic_load(ctw(p, CT_TMO), RLX_AGENT)) == p.tag)) {
      __hip_atomic_store(ctw(p, CT_TMO), p.tag, RLX_AGENT);
      return false;
    }
    __builtin_amdgcn_s_sleep(SLEEP);
  }
}
template <bool WIDE>
__device__ __forceinline__ void publish(const Ctx& p, int off, bool drain = true) {
  if (drain) WAIT_VM(0);
  BARRIER();
  if (p.w == 0 && lane_id() == 0) {
    if (WIDE) __hip_atomic_store((gu64*)ctw(p, off), tag64(p), RLX_AGENT); else __hip_atomic_store(ctw(p, off), p.tag, RLX_AGENT);
  }
}
constexpr int SLAST = NSEG - 4;
__device__ __forceinline__ v4u unsent(v4u v) { return v4u{(u32)min((int)v.x, 0x7FFFFFFE), (u32)min((int)v.y, 0x7FFFFFFE), (u32)min((int)v.z, 0x7FFFFFFE), (u32)min((int)v.w, 0x7FFFFFFE)}; }
__device__ __forceinline__ int imax4(int m, v4u v) { return max(max(m, max((int)v.x, (int)v.y)), max((int)v.z, (int)v.w)); }
template <int N>
__device__ __forceinline__ bool all_there(const v4u (&v)[N]) {
  int m = 0;
#pragma unroll
  for (int i = 0; i < N; ++i) m = imax4(m, v[i]);
  return !__any(m == (int)SENT);
}
__device__ __forceinline__ int seg_word(int s) { return min(s, 3 + (s >> 2)); }
__device__ __forceinline__ v4u ld16(__amdgpu_buffer_rsrc_t rs, int off) { return __builtin_amdgcn_raw_buffer_load_b128(rs, off, 0, SC1); }
__device__ __forceinline__ void st16(__amdgpu_buffer_rsrc_t rs, int off, v4u v) { __builtin_amdgcn_raw_buffer_store_b128(v, rs, off, 0, SC1); }

__device__ __forceinline__ int wswz(int k) { return (k & 3) | ((((k >> 2) ^ (k >> 3)) & 1) << 2); }

typedef u32 v16u __attribute__((ext_vector_type(16)));
typedef u32 v32u __attribute__((ext_vector_type(32)));
#define CLOBBER_V32_127 "v32", "v33", "v34", "v35", "v36", "v37", "v38", "v39", "v40", "v41", "v42", "v43", "v44", "v45", "v46", "v47", "v48", "v49", "v50", "v51", "v52", "v53", "v54", "v55", "v56", "v57", "v58", "v59", "v60", "v61", "v62", "v63", "v64", "v65", "v66", "v67", "v68", "v69", "v70", "v71", "v72", "v73", "v74", "v75", "v76", "v77", "v78", "v79", "v80", "v81", "v82", "v83", "v84", "v85", "v86", "v87", "v88", "v89", "v90", "v91", "v92", "v93", "v94", "v95", "v96", "v97", "v98", "v99", "v100", "v101", "v102", "v103", "v104", "v105", "v106", "v107", "v108", "v109", "v110", "v111", "v112", "v113", "v114", "v115", "v116", "v117", "v118", "v119", "v120", "v121", "v122", "v123", "v124", "v125", "v126", "v127"
#define CLOBBER_S40_63 "s40", "s41", "s42", "s43", "s44", "s45", "s46", "s47", "s48", "s49", "s50", "s51", "s52", "s53", "s54", "s55", "s56", "s57", "s58", "s59", "s60", "s61", "s62", "s63"
#define CLOBBER_V160_255 "v160", "v161", "v162", "v163", "v164", "v165", "v166", "v167", "v168", "v169", "v170", "v171", "v172", "v173", "v174", "v175", "v176", "v177", "v178", "v179", "v180", "v181", "v182", "v183", "v184", "v185", "v186", "v187", "v188", "v189", "v190", "v191", "v192", "v193", "v194", "v195", "v196", "v197", "v198", "v199", "v200", "v201", "v202", "v203", "v204", "v205", "v206", "v207", "v208", "v209", "v210", "v211", "v212", "v213", "v214", "v215", "v216", "v217", "v218", "v219", "v220", "v221", "v222", "v223", "v224", "v225", "v226", "v227", "v228", "v229", "v230", "v231", "v232", "v233", "v234", "v235", "v236", "v237", "v238", "v239", "v240", "v241", "v242", "v243", "v244", "v245", "v246", "v247", "v248", "v249", "v250", "v251", "v252", "v253", "v254", "v255"
#define STAGGER ((r0 * 4 + (j & 3)) * 3)
#define GEMM_OPERANDS \
      : "+{v[32:63]}"(xm[0]), "+{v[64:95]}"(xm[1]), "+{v[96:127]}"(xm[2]), "+{v[128:159]}"(xm[3]) \
      : [rx] "s"(rx), [nt] "s"(nt), [sg] "s"(j < 32 ? STAGGER : 0), [vw] "s"(j < 32 ? w >> 1 : 0), [fm] "s"(fm), [rd] "s"(rd), [rc] "s"(rc), [mb] "s"(lds + w * 4096), [xt] "s"(4 * 128 * DM * 2), [w] "s"(w), [act] "s"(j), \
        [s1] "s"(s1), [s2] "s"(s2), [dt] "s"(dt), [f0] "s"(f0), [fs] "s"(fs), "{v[0:15]}"(ca), \
        "{a[0:31]}"(W[0]), "{a[32:63]}"(W[1]), "{a[64:95]}"(W[2]), "{a[96:127]}"(W[3]), "{a[128:159]}"(W[4]), "{a[160:191]}"(W[5]), "{a[192:223]}"(W[6]), "{a[224:255]}"(W[7]) \
      : "memory", "scc", CLOBBER_V160_255, "s40", "s41", "s42", "s43", "s44", "s45", "s46", "s47"

__device__ __forceinline__ void proj_role(Ctx& p, __amdgpu_buffer_rsrc_t rd, u32 lds, int j, int r0) {
  const int w = p.w, lane = lane_id(), g = lane >> 4, n = lane & 15;
  const bool vwave = j < 32 && w >= 2;
  const u16 *w0, *w1;
  int ldw;
  if (j < 32) { w0 = p.wk + j * DH; w1 = p.wv + j * DH; ldw = HD; }
  else if (j < 48) { w0 = p.wq + (j - 32) * 256; w1 = w0 + 128; ldw = HD; }
  else { w0 = p.w1 + (j - 48) * 256; w1 = w0 + 128; ldw = FF; }
  v32u W[8];
  const size_t wrest = (size_t)(DM - 1) * ldw * 2 + 256;
  const __amdgpu_buffer_rsrc_t rw0 = srd(w0, wrest), rw1 = srd(w1, wrest);
  auto w_issue = [&](int kq) {
    const u32 vo = g * ldw * 2 + ((n ^ (g << 1)) << 4), k = m0_save(), base = lds + (kq & 1) * 65536 + w * 8192;
    u32 so = (kq * 128 + w * 32) * ldw * 2;
    static_for<8>([&](auto I) {
      constexpr int i = I.value, x = ((i ^ (i >> 1)) & 1) << 7;
      blds16<i * 1024, x, 0>(rw0, vo, so, base);
      blds16<32768 + i * 1024, x, 0>(rw1, vo, so, base);
      so += 8 * ldw;
    });
    m0_restore(k);
  };
  const u32 ev = epoch_ask(p);
  w_issue(0);
  w_issue(1);
  if (j < 32) TELL_XCD(ev, 32);
#pragma unroll
  for (int kq = 0; kq < 4; ++kq) {
    if (kq < 3) WAIT_VM(16); else WAIT_VM(8);
    BARRIER();
    if (kq == 3) adopt_tag(p, ev, j < 32);
#pragma unroll
    for (int hf = 0; hf < 2; ++hf) {
      const int k = 8 * g + 4 * hf + (n >> 2);
#pragma unroll
      for (int t = 0; t < 4; ++t) {
        const u32 a = lds + (kq & 1) * 65536 + (w >> 1) * 32768 + k * 256 + ((((w & 1) * 4 + t) ^ wswz(k)) << 5) + (n & 3) * 8;
#pragma unroll
        for (int ks = 0; ks < 4; ++ks) {
          const v2u v = trread(a + ks * 8192);
          W[kq * 2 + (ks >> 1)][((ks & 1) * 4 + t) * 4 + 2 * hf] = v.x;
          W[kq * 2 + (ks >> 1)][((ks & 1) * 4 + t) * 4 + 2 * hf + 1] = v.y;
        }
      }
    }
    WAIT_LGKM0();
    BARRIER();
    if (kq < 2) w_issue(kq + 2);
    if (kq == 2) {
#pragma unroll
      for (int i = 0; i < 8; ++i)
        glds16(p.x + (size_t)(r0 * 32 + w * 8 + (lane >> 3)) * DM + i * 64 + (((lane & 7) ^ (lane >> 3)) << 3), lds + i * 4096 + w * 1024);
    }
    if (kq == 0) asm volatile("" : "+{a[0:31]}"(W[0]), "+{a[32:63]}"(W[1]));
    if (kq == 1) asm volatile("" : "+{a[64:95]}"(W[2]), "+{a[96:127]}"(W[3]));
    if (kq == 2) asm volatile("" : "+{a[128:159]}"(W[4]), "+{a[160:191]}"(W[5]));
    if (kq == 3) asm volatile("" : "+{a[192:223]}"(W[6]), "+{a[224:255]}"(W[7]));
  }
  WAIT_VM(0);
  BARRIER();
  v32u xm[4];
#pragma unroll
  for (int i = 0; i < 32; ++i) {
    const v4u v = *(const LAS v4u*)(lds + ((i & 15) >> 1) * 4096 + ((i >> 4) * 16 + n) * 128 + (((4 * (i & 1) + g) ^ (n & 7)) << 4));
#pragma unroll
    for (int d = 0; d < 4; ++d) xm[i >> 3][(i & 7) * 4 + d] = v[d];
  }
  WAIT_LGKM0();
  BARRIER();
  const int r1 = r0 + 1, nt = r0 == 3 ? 3 : 4;
  const __amdgpu_buffer_rsrc_t rx = srd(p.x + (size_t)r1 * 128 * DM, (size_t)(SEQ - r1 * 128) * DM * 2);
  const __amdgpu_buffer_rsrc_t rc = srd(p.ws + WS_CTRL, CT_BYTES);
  const int act = j >= 48, sg = act ? 2 * 56 * 1024 : AB_BLK;
  int dst, s1, s2;
  if (vwave) { dst = j * NSEG * AB_BLK + AB_V + (w - 2) * 4096; s1 = 0; s2 = AB_BLK; }
  else if (j < 32) { dst = j * NSEG * AB_BLK + AB_K + 2 * w * 1024; s1 = 4096; s2 = AB_BLK - 4096; }
  else if (j < 48) { dst = (2 * (j - 32) + (w >> 1)) * NSEG * AB_BLK + AB_Q + 2 * (w & 1) * 1024; s1 = 4096; s2 = AB_BLK - 4096; }
  else { dst = RD_HF + (8 * (j - 48) + 2 * w) * 1024; s1 = 56 * 1024; s2 = 56 * 1024; }
  const int dt = 16 * sg, fs = act ? 128 : 16, fb = act ? CT_H + (j - 48) * 4 : CT_KV + j * 128;
  const int fm = fb + r0 * (fs >> 2), f0 = fb + (3 + r1) * (fs >> 2);
  v16u ca;
#pragma unroll
  for (int i = 0; i < 4; ++i) {
    const int r = lane >> 3;
    ca[i] = ((32 * w + 8 * i + r) * DM + (((lane & 7) ^ r) << 3)) * 2;
    ca[4 + i] = lds + (i & 1) * 65536 + n * 128 + (((4 * (i >> 1) + g) ^ (n & 7)) << 4);
  }
  ca[8] = dst + r1 * 4 * sg + lane * 16;
  ca[9] = p.tag;
  ca[10] = dst + r0 * sg + lane * 16;
#pragma unroll
  for (int i = 11; i < 16; ++i) ca[i] = 1;
  if (j < 32) TLS(p.b * 8 + 2);
  asm volatile(
    "s_cmp_lg_u32 %[vw], 0\n\t"
    "s_cbranch_scc1 8f\n\t"
    "s_mov_b32 s42, m0\n\t"
    "s_mov_b32 s41, 0\n\t"
    "s_mov_b32 s40, %[nt]\n\t"
    "s_mov_b32 s43, 0\n\t"
    "s_mov_b32 s44, 0x40000000\n\t"
    "s_mov_b32 s47, %[f0]\n\t"
    "v_mfma_f32_16x16x32_bf16 v[160:163], a[0:3], v[32:35], 0\n\t"
    "v_mfma_f32_16x16x32_bf16 v[164:167], a[4:7], v[32:35], 0\n\t"
    "v_mfma_f32_16x16x32_bf16 v[168:171], a[8:11], v[32:35], 0\n\t"
    "v_mfma_f32_16x16x32_bf16 v[172:175], a[12:15], v[32:35], 0\n\t"
    "v_mfma_f32_16x16x32_bf16 v[176:179], a[0:3], v[96:99], 0\n\t"
    "v_mfma_f32_16x16x32_bf16 v[180:183], a[4:7], v[96:99], 0\n\t"
    "v_mfma_f32_16x16x32_bf16 v[184:187], a[8:11], v[96:99], 0\n\t"
    "v_mfma_f32_16x16x32_bf16 v[188:191], a[12:15], v[96:99], 0\n\t"
    "v_mfma_f32_16x16x32_bf16 v[160:163], a[16:19], v[36:39], v[160:163]\n\t"
    "v_mfma_f32_16x16x32_bf16 v[164:167], a[20:23], v[36:39], v[164:167]\n\t"
    "v_mfma_f32_16x16x32_bf16 v[168:171], a[24:27], v[36:39], v[168:171]\n\t"
    "v_mfma_f32_16x16x32_bf16 v[172:175], a[28:31], v[36:39], v[172:175]\n\t"
    "v_mfma_f32_16x16x32_bf16 v[176:179], a[16:19], v[100:103], v[176:179]\n\t"
    "v_mfma_f32_16x16x32_bf16 v[180:183], a[20:23], v[100:103], v[180:183]\n\t"
    "v_mfma_f32_16x16x32_bf16 v[184:187], a[24:27], v[100:103], v[184:187]\n\t"
    "v_mfma_f32_16x16x32_bf16 v[188:191], a[28:31], v[100:103], v[188:191]\n\t"
    "v_mfma_f32_16x16x32_bf16 v[160:163], a[32:35], v[40:43], v[160:163]\n\t"
    "v_mfma_f32_16x16x32_bf16 v[164:167], a[36:39], v[40:43], v[164:167]\n\t"
    "v_mfma_f32_16x16x32_bf16 v[168:171], a[40:43], v[40:43], v[168:171]\n\t"
    "v_mfma_f32_16x16x32_bf16 v[172:175], a[44:47], v[40:43], v[172:175]\n\t"
    "v_mfma_f32_16x16x32_bf16 v[176:179], a[32:35], v[104:107], v[176:179]\n\t"
    "v_mfma_f32_16x16x32_bf16 v[180:183], a[36:39], v[104:107], v[180:183]\n\t"
    "v_mfma_f32_16x16x32_bf16 v[184:187], a[40:43], v[104:107], v[184:187]\n\t"
    "v_mfma_f32_16x16x32_bf16 v[188:191], a[44:47], v[104:107], v[188:191]\n\t"
    "v_mfma_f32_16x16x32_bf16 v[160:163], a[48:51], v[44:47], v[160:163]\n\t"
    "v_mfma_f32_16x16x32_bf16 v[164:167], a[52:55], v[44:47], v[164:167]\n\t"
    "v_mfma_f32_16x16x32_bf16 v[168:171], a[56:59], v[44:47], v[168:171]\n\t"
    "v_mfma_f32_16x16x32_bf16 v[172:175], a[60:63], v[44:47], v[172:175]\n\t"
    "v_mfma_f32_16x16x32_bf16 v[176:179], a[48:51], v[108:111], v[176:179]\n\t"
    "v_mfma_f32_16x16x32_bf16 v[180:183], a[52:55], v[108:111], v[180:183]\n\t"
    "v_mfma_f32_16x16x32_bf16 v[184:187], a[56:59], v[108:111], v[184:187]\n\t"
    "v_mfma_f32_16x16x32_bf16 v[188:191], a[60:63], v[108:111], v[188:191]\n\t"
    "v_mfma_f32_16x16x32_bf16 v[160:163], a[64:67], v[48:51], v[160:163]\n\t"
    "v_mfma_f32_16x16x32_bf16 v[164:167], a[68:71], v[48:51], v[164:167]\n\t"
    "v_mfma_f32_16x16x32_bf16 v[168:171], a[72:75], v[48:51], v[168:171]\n\t"
    "v_mfma_f32_16x16x32_bf16 v[172:175], a[76:79], v[48:51], v[172:175]\n\t"
    "v_mfma_f32_16x16x32_bf16 v[176:179], a[64:67], v[112:115], v[176:179]\n\t"
    "v_mfma_f32_16x16x32_bf16 v[180:183], a[68:71], v[112:115], v[180:183]\n\t"
    "v_mfma_f32_16x16x32_bf16 v[184:187], a[72:75], v[112:115], v[184:187]\n\t"
    "v_mfma_f32_16x16x32_bf16 v[188:191], a[76:79], v[112:115], v[188:191]\n\t"
    "v_mfma_f32_16x16x32_bf16 v[160:163], a[80:83], v[52:55], v[160:163]\n\t"
    "v_mfma_f32_16x16x32_bf16 v[164:167], a[84:87], v[52:55], v[164:167]\n\t"
    "v_mfma_f32_16x16x32_bf16 v[168:171], a[88:91], v[52:55], v[168:171]\n\t"
    "v_mfma_f32_16x16x32_bf16 v[172:175], a[92:95], v[52:55], v[172:175]\n\t"
    "v_mfma_f32_16x16x32_bf16 v[176:179], a[80:83], v[116:119], v[176:179]\n\t"
    "v_mfma_f32_16x16x32_bf16 v[180:183], a[84:87], v[116:119], v[180:183]\n\t"
    "v_mfma_f32_16x16x32_bf16 v[184:187], a[88:91], v[116:119], v[184:187]\n\t"
    "v_mfma_f32_16x16x32_bf16 v[188:191], a[92:95], v[116:119], v[188:191]\n\t"
    "v_mfma_f32_16x16x32_bf16 v[160:163], a[96:99], v[56:59], v[160:163]\n\t"
    "v_mfma_f32_16x16x32_bf16 v[164:167], a[100:103], v[56:59], v[164:167]\n\t"
    "v_mfma_f32_16x16x32_bf16 v[168:171], a[104:107], v[56:59], v[168:171]\n\t"
    "v_mfma_f32_16x16x32_bf16 v[172:175], a[108:111], v[56:59], v[172:175]\n\t"
    "v_mfma_f32_16x16x32_bf16 v[176:179], a[96:99], v[120:123], v[176:179]\n\t"
    "v_mfma_f32_16x16x32_bf16 v[180:183], a[100:103], v[120:123], v[180:183]\n\t"
    "v_mfma_f32_16x16x32_bf16 v[184:187], a[104:107], v[120:123], v[184:187]\n\t"
    "v_mfma_f32_16x16x32_bf16 v[188:191], a[108:111], v[120:123], v[188:191]\n\t"
    "v_mfma_f32_16x16x32_bf16 v[160:163], a[112:115], v[60:63], v[160:163]\n\t"
    "v_mfma_f32_16x16x32_bf16 v[164:167], a[116:119], v[60:63], v[164:167]\n\t"
    "v_mfma_f32_16x16x32_bf16 v[168:171], a[120:123], v[60:63], v[168:171]\n\t"
    "v_mfma_f32_16x16x32_bf16 v[172:175], a[124:127], v[60:63], v[172:175]\n\t"
    "v_mfma_f32_16x16x32_bf16 v[176:179], a[112:115], v[124:127], v[176:179]\n\t"
    "v_mfma_f32_16x16x32_bf16 v[180:183], a[116:119], v[124:127], v[180:183]\n\t"
    "v_mfma_f32_16x16x32_bf16 v[184:187], a[120:123], v[124:127], v[184:187]\n\t"
    "v_mfma_f32_16x16x32_bf16 v[188:191], a[124:127], v[124:127], v[188:191]\n\t"
    "v_mfma_f32_16x16x32_bf16 v[160:163], a[128:131], v[64:67], v[160:163]\n\t"
    "v_mfma_f32_16x16x32_bf16 v[164:167], a[132:135], v[64:67], v[164:167]\n\t"
    "v_mfma_f32_16x16x32_bf16 v[168:171], a[136:139], v[64:67], v[168:171]\n\t"
    "v_mfma_f32_16x16x32_bf16 v[172:175], a[140:143], v[64:67], v[172:175]\n\t"
    "v_mfma_f32_16x16x32_bf16 v[176:179], a[128:131], v[128:131], v[176:179]\n\t"
    "v_mfma_f32_16x16x32_bf16 v[180:183], a[132:135], v[128:131], v[180:183]\n\t"
    "v_mfma_f32_16x16x32_bf16 v[184:187], a[136:139], v[128:131], v[184:187]\n\t"
    "v_mfma_f32_16x16x32_bf16 v[188:191], a[140:143], v[128:131], v[188:191]\n\t"
    "v_mfma_f32_16x16x32_bf16 v[160:163], a[144:147], v[68:71], v[160:163]\n\t"
    "v_mfma_f32_16x16x32_bf16 v[164:167], a[148:151], v[68:71], v[164:167]\n\t"
    "v_mfma_f32_16x16x32_bf16 v[168:171], a[152:155], v[68:71], v[168:171]\n\t"
    "v_mfma_f32_16x16x32_bf16 v[172:175], a[156:159], v[68:71], v[172:175]\n\t"
    "v_mfma_f32_16x16x32_bf16 v[176:179], a[144:147], v[132:135], v[176:179]\n\t"
    "v_mfma_f32_16x16x32_bf16 v[180:183], a[148:151], v[132:135], v[180:183]\n\t"
    "v_mfma_f32_16x16x32_bf16 v[184:187], a[152:155], v[132:135], v[184:187]\n\t"
    "v_mfma_f32_16x16x32_bf16 v[188:191], a[156:159], v[132:135], v[188:191]\n\t"
    "v_mfma_f32_16x16x32_bf16 v[160:163], a[160:163], v[72:75], v[160:163]\n\t"
    "v_mfma_f32_16x16x32_bf16 v[164:167], a[164:167], v[72:75], v[164:167]\n\t"
    "v_mfma_f32_16x16x32_bf16 v[168:171], a[168:171], v[72:75], v[168:171]\n\t"
    "v_mfma_f32_16x16x32_bf16 v[172:175], a[172:175], v[72:75], v[172:175]\n\t"
    "v_mfma_f32_16x16x32_bf16 v[176:179], a[160:163], v[136:139], v[176:179]\n\t"
    "v_mfma_f32_16x16x32_bf16 v[180:183], a[164:167], v[136:139], v[180:183]\n\t"
    "v_mfma_f32_16x16x32_bf16 v[184:187], a[168:171], v[136:139], v[184:187]\n\t"
    "v_mfma_f32_16x16x32_bf16 v[188:191], a[172:175], v[136:139], v[188:191]\n\t"
    "v_mfma_f32_16x16x32_bf16 v[160:163], a[176:179], v[76:79], v[160:163]\n\t"
    "v_mfma_f32_16x16x32_bf16 v[164:167], a[180:183], v[76:79], v[164:167]\n\t"
    "v_mfma_f32_16x16x32_bf16 v[168:171], a[184:187], v[76:79], v[168:171]\n\t"
    "v_mfma_f32_16x16x32_bf16 v[172:175], a[188:191], v[76:79], v[172:175]\n\t"
    "v_mfma_f32_16x16x32_bf16 v[176:179], a[176:179], v[140:143], v[176:179]\n\t"
    "v_mfma_f32_16x16x32_bf16 v[180:183], a[180:183], v[140:143], v[180:183]\n\t"
    "v_mfma_f32_16x16x32_bf16 v[184:187], a[184:187], v[140:143], v[184:187]\n\t"
    "v_mfma_f32_16x16x32_bf16 v[188:191], a[188:191], v[140:143], v[188:191]\n\t"
    "v_mfma_f32_16x16x32_bf16 v[160:163], a[192:195], v[80:83], v[160:163]\n\t"
    "v_mfma_f32_16x16x32_bf16 v[164:167], a[196:199], v[80:83], v[164:167]\n\t"
    "v_mfma_f32_16x16x32_bf16 v[168:171], a[200:203], v[80:83], v[168:171]\n\t"
    "v_mfma_f32_16x16x32_bf16 v[172:175], a[204:207], v[80:83], v[172:175]\n\t"
    "v_mfma_f32_16x16x32_bf16 v[176:179], a[192:195], v[144:147], v[176:179]\n\t"
    "v_mfma_f32_16x16x32_bf16 v[180:183], a[196:199], v[144:147], v[180:183]\n\t"
    "v_mfma_f32_16x16x32_bf16 v[184:187], a[200:203], v[144:147], v[184:187]\n\t"
    "v_mfma_f32_16x16x32_bf16 v[188:191], a[204:207], v[144:147], v[188:191]\n\t"
    "v_mfma_f32_16x16x32_bf16 v[160:163], a[208:211], v[84:87], v[160:163]\n\t"
    "v_mfma_f32_16x16x32_bf16 v[164:167], a[212:215], v[84:87], v[164:167]\n\t"
    "v_mfma_f32_16x16x32_bf16 v[168:171], a[216:219], v[84:87], v[168:171]\n\t"
    "v_mfma_f32_16x16x32_bf16 v[172:175], a[220:223], v[84:87], v[172:175]\n\t"
    "v_mfma_f32_16x16x32_bf16 v[176:179], a[208:211], v[148:151], v[176:179]\n\t"
    "v_mfma_f32_16x16x32_bf16 v[180:183], a[212:215], v[148:151], v[180:183]\n\t"
    "v_mfma_f32_16x16x32_bf16 v[184:187], a[216:219], v[148:151], v[184:187]\n\t"
    "v_mfma_f32_16x16x32_bf16 v[188:191], a[220:223], v[148:151], v[188:191]\n\t"
    "v_mfma_f32_16x16x32_bf16 v[160:163], a[224:227], v[88:91], v[160:163]\n\t"
    "v_mfma_f32_16x16x32_bf16 v[164:167], a[228:231], v[88:91], v[164:167]\n\t"
    "v_mfma_f32_16x16x32_bf16 v[168:171], a[232:235], v[88:91], v[168:171]\n\t"
    "v_mfma_f32_16x16x32_bf16 v[172:175], a[236:239], v[88:91], v[172:175]\n\t"
    "v_mfma_f32_16x16x32_bf16 v[176:179], a[224:227], v[152:155], v[176:179]\n\t"
    "v_mfma_f32_16x16x32_bf16 v[180:183], a[228:231], v[152:155], v[180:183]\n\t"
    "v_mfma_f32_16x16x32_bf16 v[184:187], a[232:235], v[152:155], v[184:187]\n\t"
    "v_mfma_f32_16x16x32_bf16 v[188:191], a[236:239], v[152:155], v[188:191]\n\t"
    "v_mfma_f32_16x16x32_bf16 v[160:163], a[240:243], v[92:95], v[160:163]\n\t"
    "v_mfma_f32_16x16x32_bf16 v[164:167], a[244:247], v[92:95], v[164:167]\n\t"
    "v_mfma_f32_16x16x32_bf16 v[168:171], a[248:251], v[92:95], v[168:171]\n\t"
    "v_mfma_f32_16x16x32_bf16 v[172:175], a[252:255], v[92:95], v[172:175]\n\t"
    "v_mfma_f32_16x16x32_bf16 v[176:179], a[240:243], v[156:159], v[176:179]\n\t"
    "v_mfma_f32_16x16x32_bf16 v[180:183], a[244:247], v[156:159], v[180:183]\n\t"
    "v_mfma_f32_16x16x32_bf16 v[184:187], a[248:251], v[156:159], v[184:187]\n\t"
    "v_mfma_f32_16x16x32_bf16 v[188:191], a[252:255], v[156:159], v[188:191]\n\t"
    "s_cmp_lt_u32 %[act], 48\n\t"
    "s_cbranch_scc1 3f\n\t"
    "s_nop 7\n\t"
    "v_cvt_pk_bf16_f32 v192, v160, v161\n\t"
    "v_lshlrev_b32 v160, 16, v192\n\t"
    "v_and_b32 v161, 0xffff0000, v192\n\t"
    "v_mul_f32 v193, 0xbfb8aa3b, v160\n\t"
    "v_mul_f32 v194, 0xbfb8aa3b, v161\n\t"
    "v_exp_f32 v193, v193\n\t"
    "v_exp_f32 v194, v194\n\t"
    "v_add_f32 v193, 1.0, v193\n\t"
    "v_add_f32 v194, 1.0, v194\n\t"
    "v_rcp_f32 v193, v193\n\t"
    "v_rcp_f32 v194, v194\n\t"
    "v_mul_f32 v160, v160, v193\n\t"
    "v_mul_f32 v161, v161, v194\n\t"
    "v_cvt_pk_bf16_f32 v192, v162, v163\n\t"
    "v_lshlrev_b32 v162, 16, v192\n\t"
    "v_and_b32 v163, 0xffff0000, v192\n\t"
    "v_mul_f32 v193, 0xbfb8aa3b, v162\n\t"
    "v_mul_f32 v194, 0xbfb8aa3b, v163\n\t"
    "v_exp_f32 v193, v193\n\t"
    "v_exp_f32 v194, v194\n\t"
    "v_add_f32 v193, 1.0, v193\n\t"
    "v_add_f32 v194, 1.0, v194\n\t"
    "v_rcp_f32 v193, v193\n\t"
    "v_rcp_f32 v194, v194\n\t"
    "v_mul_f32 v162, v162, v193\n\t"
    "v_mul_f32 v163, v163, v194\n\t"
    "v_cvt_pk_bf16_f32 v192, v164, v165\n\t"
    "v_lshlrev_b32 v164, 16, v192\n\t"
    "v_and_b32 v165, 0xffff0000, v192\n\t"
    "v_mul_f32 v193, 0xbfb8aa3b, v164\n\t"
    "v_mul_f32 v194, 0xbfb8aa3b, v165\n\t"
    "v_exp_f32 v193, v193\n\t"
    "v_exp_f32 v194, v194\n\t"
    "v_add_f32 v193, 1.0, v193\n\t"
    "v_add_f32 v194, 1.0, v194\n\t"
    "v_rcp_f32 v193, v193\n\t"
    "v_rcp_f32 v194, v194\n\t"
    "v_mul_f32 v164, v164, v193\n\t"
    "v_mul_f32 v165, v165, v194\n\t"
    "v_cvt_pk_bf16_f32 v192, v166, v167\n\t"
    "v_lshlrev_b32 v166, 16, v192\n\t"
    "v_and_b32 v167, 0xffff0000, v192\n\t"
    "v_mul_f32 v193, 0xbfb8aa3b, v166\n\t"
    "v_mul_f32 v194, 0xbfb8aa3b, v167\n\t"
    "v_exp_f32 v193, v193\n\t"
    "v_exp_f32 v194, v194\n\t"
    "v_add_f32 v193, 1.0, v193\n\t"
    "v_add_f32 v194, 1.0, v194\n\t"
    "v_rcp_f32 v193, v193\n\t"
    "v_rcp_f32 v194, v194\n\t"
    "v_mul_f32 v166, v166, v193\n\t"
    "v_mul_f32 v167, v167, v194\n\t"
    "v_cvt_pk_bf16_f32 v192, v168, v169\n\t"
    "v_lshlrev_b32 v168, 16, v192\n\t"
    "v_and_b32 v169, 0xffff0000, v192\n\t"
    "v_mul_f32 v193, 0xbfb8aa3b, v168\n\t"
    "v_mul_f32 v194, 0xbfb8aa3b, v169\n\t"
    "v_exp_f32 v193, v193\n\t"
    "v_exp_f32 v194, v194\n\t"
    "v_add_f32 v193, 1.0, v193\n\t"
    "v_add_f32 v194, 1.0, v194\n\t"
    "v_rcp_f32 v193, v193\n\t"
    "v_rcp_f32 v194, v194\n\t"
    "v_mul_f32 v168, v168, v193\n\t"
    "v_mul_f32 v169, v169, v194\n\t"
    "v_cvt_pk_bf16_f32 v192, v170, v171\n\t"
    "v_lshlrev_b32 v170, 16, v192\n\t"
    "v_and_b32 v171, 0xffff0000, v192\n\t"
    "v_mul_f32 v193, 0xbfb8aa3b, v170\n\t"
    "v_mul_f32 v194, 0xbfb8aa3b, v171\n\t"
    "v_exp_f32 v193, v193\n\t"
    "v_exp_f32 v194, v194\n\t"
    "v_add_f32 v193, 1.0, v193\n\t"
    "v_add_f32 v194, 1.0, v194\n\t"
    "v_rcp_f32 v193, v193\n\t"
    "v_rcp_f32 v194, v194\n\t"
    "v_mul_f32 v170, v170, v193\n\t"
    "v_mul_f32 v171, v171, v194\n\t"
    "v_cvt_pk_bf16_f32 v192, v172, v173\n\t"
    "v_lshlrev_b32 v172, 16, v192\n\t"
    "v_and_b32 v173, 0xffff0000, v192\n\t"
    "v_mul_f32 v193, 0xbfb8aa3b, v172\n\t"
    "v_mul_f32 v194, 0xbfb8aa3b, v173\n\t"
    "v_exp_f32 v193, v193\n\t"
    "v_exp_f32 v194, v194\n\t"
    "v_add_f32 v193, 1.0, v193\n\t"
    "v_add_f32 v194, 1.0, v194\n\t"
    "v_rcp_f32 v193, v193\n\t"
    "v_rcp_f32 v194, v194\n\t"
    "v_mul_f32 v172, v172, v193\n\t"
    "v_mul_f32 v173, v173, v194\n\t"
    "v_cvt_pk_bf16_f32 v192, v174, v175\n\t"
    "v_lshlrev_b32 v174, 16, v192\n\t"
    "v_and_b32 v175, 0xffff0000, v192\n\t"
    "v_mul_f32 v193, 0xbfb8aa3b, v174\n\t"
    "v_mul_f32 v194, 0xbfb8aa3b, v175\n\t"
    "v_exp_f32 v193, v193\n\t"
    "v_exp_f32 v194, v194\n\t"
    "v_add_f32 v193, 1.0, v193\n\t"
    "v_add_f32 v194, 1.0, v194\n\t"
    "v_rcp_f32 v193, v193\n\t"
    "v_rcp_f32 v194, v194\n\t"
    "v_mul_f32 v174, v174, v193\n\t"
    "v_mul_f32 v175, v175, v194\n\t"
    "v_cvt_pk_bf16_f32 v192, v176, v177\n\t"
    "v_lshlrev_b32 v176, 16, v192\n\t"
    "v_and_b32 v177, 0xffff0000, v192\n\t"
    "v_mul_f32 v193, 0xbfb8aa3b, v176\n\t"
    "v_mul_f32 v194, 0xbfb8aa3b, v177\n\t"
    "v_exp_f32 v193, v193\n\t"
    "v_exp_f32 v194, v194\n\t"
    "v_add_f32 v193, 1.0, v193\n\t"
    "v_add_f32 v194, 1.0, v194\n\t"
    "v_rcp_f32 v193, v193\n\t"
    "v_rcp_f32 v194, v194\n\t"
    "v_mul_f32 v176, v176, v193\n\t"
    "v_mul_f32 v177, v177, v194\n\t"
    "v_cvt_pk_bf16_f32 v192, v178, v179\n\t"
    "v_lshlrev_b32 v178, 16, v192\n\t"
    "v_and_b32 v179, 0xffff0000, v192\n\t"
    "v_mul_f32 v193, 0xbfb8aa3b, v178\n\t"
    "v_mul_f32 v194, 0xbfb8aa3b, v179\n\t"
    "v_exp_f32 v193, v193\n\t"
    "v_exp_f32 v194, v194\n\t"
    "v_add_f32 v193, 1.0, v193\n\t"
    "v_add_f32 v194, 1.0, v194\n\t"
    "v_rcp_f32 v193, v193\n\t"
    "v_rcp_f32 v194, v194\n\t"
    "v_mul_f32 v178, v178, v193\n\t"
    "v_mul_f32 v179, v179, v194\n\t"
    "v_cvt_pk_bf16_f32 v192, v180, v181\n\t"
    "v_lshlrev_b32 v180, 16, v192\n\t"
    "v_and_b32 v181, 0xffff0000, v192\n\t"
    "v_mul_f32 v193, 0xbfb8aa3b, v180\n\t"
    "v_mul_f32 v194, 0xbfb8aa3b, v181\n\t"
    "v_exp_f32 v193, v193\n\t"
    "v_exp_f32 v194, v194\n\t"
    "v_add_f32 v193, 1.0, v193\n\t"
    "v_add_f32 v194, 1.0, v194\n\t"
    "v_rcp_f32 v193, v193\n\t"
    "v_rcp_f32 v194, v194\n\t"
    "v_mul_f32 v180, v180, v193\n\t"
    "v_mul_f32 v181, v181, v194\n\t"
    "v_cvt_pk_bf16_f32 v192, v182, v183\n\t"
    "v_lshlrev_b32 v182, 16, v192\n\t"
    "v_and_b32 v183, 0xffff0000, v192\n\t"
    "v_mul_f32 v193, 0xbfb8aa3b, v182\n\t"
    "v_mul_f32 v194, 0xbfb8aa3b, v183\n\t"
    "v_exp_f32 v193, v193\n\t"
    "v_exp_f32 v194, v194\n\t"
    "v_add_f32 v193, 1.0, v193\n\t"
    "v_add_f32 v194, 1.0, v194\n\t"
    "v_rcp_f32 v193, v193\n\t"
    "v_rcp_f32 v194, v194\n\t"
    "v_mul_f32 v182, v182, v193\n\t"
    "v_mul_f32 v183, v183, v194\n\t"
    "v_cvt_pk_bf16_f32 v192, v184, v185\n\t"
    "v_lshlrev_b32 v184, 16, v192\n\t"
    "v_and_b32 v185, 0xffff0000, v192\n\t"
    "v_mul_f32 v193, 0xbfb8aa3b, v184\n\t"
    "v_mul_f32 v194, 0xbfb8aa3b, v185\n\t"
    "v_exp_f32 v193, v193\n\t"
    "v_exp_f32 v194, v194\n\t"
    "v_add_f32 v193, 1.0, v193\n\t"
    "v_add_f32 v194, 1.0, v194\n\t"
    "v_rcp_f32 v193, v193\n\t"
    "v_rcp_f32 v194, v194\n\t"
    "v_mul_f32 v184, v184, v193\n\t"
    "v_mul_f32 v185, v185, v194\n\t"
    "v_cvt_pk_bf16_f32 v192, v186, v187\n\t"
    "v_lshlrev_b32 v186, 16, v192\n\t"
    "v_and_b32 v187, 0xffff0000, v192\n\t"
    "v_mul_f32 v193, 0xbfb8aa3b, v186\n\t"
    "v_mul_f32 v194, 0xbfb8aa3b, v187\n\t"
    "v_exp_f32 v193, v193\n\t"
    "v_exp_f32 v194, v194\n\t"
    "v_add_f32 v193, 1.0, v193\n\t"
    "v_add_f32 v194, 1.0, v194\n\t"
    "v_rcp_f32 v193, v193\n\t"
    "v_rcp_f32 v194, v194\n\t"
    "v_mul_f32 v186, v186, v193\n\t"
    "v_mul_f32 v187, v187, v194\n\t"
    "v_cvt_pk_bf16_f32 v192, v188, v189\n\t"
    "v_lshlrev_b32 v188, 16, v192\n\t"
    "v_and_b32 v189, 0xffff0000, v192\n\t"
    "v_mul_f32 v193, 0xbfb8aa3b, v188\n\t"
    "v_mul_f32 v194, 0xbfb8aa3b, v189\n\t"
    "v_exp_f32 v193, v193\n\t"
    "v_exp_f32 v194, v194\n\t"
    "v_add_f32 v193, 1.0, v193\n\t"
    "v_add_f32 v194, 1.0, v194\n\t"
    "v_rcp_f32 v193, v193\n\t"
    "v_rcp_f32 v194, v194\n\t"
    "v_mul_f32 v188, v188, v193\n\t"
    "v_mul_f32 v189, v189, v194\n\t"
    "v_cvt_pk_bf16_f32 v192, v190, v191\n\t"
    "v_lshlrev_b32 v190, 16, v192\n\t"
    "v_and_b32 v191, 0xffff0000, v192\n\t"
    "v_mul_f32 v193, 0xbfb8aa3b, v190\n\t"
    "v_mul_f32 v194, 0xbfb8aa3b, v191\n\t"
    "v_exp_f32 v193, v193\n\t"
    "v_exp_f32 v194, v194\n\t"
    "v_add_f32 v193, 1.0, v193\n\t"
    "v_add_f32 v194, 1.0, v194\n\t"
    "v_rcp_f32 v193, v193\n\t"
    "v_rcp_f32 v194, v194\n\t"
    "v_mul_f32 v190, v190, v193\n\t"
    "v_mul_f32 v191, v191, v194\n\t"
    "3:\n\t"
    "s_nop 7\n\t"
    "v_cvt_pk_bf16_f32 v192, v160, v161\n\t"
    "v_cvt_pk_bf16_f32 v193, v162, v163\n\t"
    "v_cvt_pk_bf16_f32 v194, v164, v165\n\t"
    "v_cvt_pk_bf16_f32 v195, v166, v167\n\t"
    "buffer_store_dwordx4 v[192:195], v10, %[rd], 0 offen offset:0 sc1\n\t"
    "v_cvt_pk_bf16_f32 v196, v168, v169\n\t"
    "v_cvt_pk_bf16_f32 v197, v170, v171\n\t"
    "v_cvt_pk_bf16_f32 v198, v172, v173\n\t"
    "v_cvt_pk_bf16_f32 v199, v174, v175\n\t"
    "buffer_store_dwordx4 v[196:199], v10, %[rd], 0 offen offset:1024 sc1\n\t"
    "v_cvt_pk_bf16_f32 v200, v176, v177\n\t"
    "v_cvt_pk_bf16_f32 v201, v178, v179\n\t"
    "v_cvt_pk_bf16_f32 v202, v180, v181\n\t"
    "v_cvt_pk_bf16_f32 v203, v182, v183\n\t"
    "buffer_store_dwordx4 v[200:203], v10, %[rd], %[s1] offen offset:0 sc1\n\t"
    "v_cvt_pk_bf16_f32 v204, v184, v185\n\t"
    "v_cvt_pk_bf16_f32 v205, v186, v187\n\t"
    "v_cvt_pk_bf16_f32 v206, v188, v189\n\t"
    "v_cvt_pk_bf16_f32 v207, v190, v191\n\t"
    "buffer_store_dwordx4 v[204:207], v10, %[rd], %[s1] offen offset:1024 sc1\n\t"
    "s_add_u32 s46, s41, 0\n\t"
    "s_add_u32 m0, %[mb], 0\n\t"
    "s_nop 0\n\t"
    "buffer_load_dwordx4 v0, %[rx], s46 offen lds\n\t"
    "s_add_u32 m0, %[mb], 1024\n\t"
    "s_nop 0\n\t"
    "buffer_load_dwordx4 v1, %[rx], s46 offen lds\n\t"
    "s_add_u32 m0, %[mb], 2048\n\t"
    "s_nop 0\n\t"
    "buffer_load_dwordx4 v2, %[rx], s46 offen lds\n\t"
    "s_add_u32 m0, %[mb], 3072\n\t"
    "s_nop 0\n\t"
    "buffer_load_dwordx4 v3, %[rx], s46 offen lds\n\t"
    "s_add_u32 s46, s41, 128\n\t"
    "s_add_u32 m0, %[mb], 16384\n\t"
    "s_nop 0\n\t"
    "buffer_load_dwordx4 v0, %[rx], s46 offen lds\n\t"
    "s_add_u32 m0, %[mb], 17408\n\t"
    "s_nop 0\n\t"
    "buffer_load_dwordx4 v1, %[rx], s46 offen lds\n\t"
    "s_add_u32 m0, %[mb], 18432\n\t"
    "s_nop 0\n\t"
    "buffer_load_dwordx4 v2, %[rx], s46 offen lds\n\t"
    "s_add_u32 m0, %[mb], 19456\n\t"
    "s_nop 0\n\t"
    "buffer_load_dwordx4 v3, %[rx], s46 offen lds\n\t"
    "s_waitcnt vmcnt(8)\n\t"
    "s_barrier\n\t"
    "s_cmp_lg_u32 %[w], 0\n\t"
    "s_cbranch_scc1 2f\n\t"
    "s_mov_b64 exec, 1\n\t"
    "buffer_store_dword v9, off, %[rc], %[fm] sc1\n\t"
    "s_mov_b64 exec, -1\n\t"
    "2:\n\t"
    "s_add_u32 s46, s41, 256\n\t"
    "s_add_u32 m0, %[mb], 32768\n\t"
    "s_nop 0\n\t"
    "buffer_load_dwordx4 v0, %[rx], s46 offen lds\n\t"
    "s_add_u32 m0, %[mb], 33792\n\t"
    "s_nop 0\n\t"
    "buffer_load_dwordx4 v1, %[rx], s46 offen lds\n\t"
    "s_add_u32 m0, %[mb], 34816\n\t"
    "s_nop 0\n\t"
    "buffer_load_dwordx4 v2, %[rx], s46 offen lds\n\t"
    "s_add_u32 m0, %[mb], 35840\n\t"
    "s_nop 0\n\t"
    "buffer_load_dwordx4 v3, %[rx], s46 offen lds\n\t"
    "s_add_u32 s46, s41, 384\n\t"
    "s_add_u32 m0, %[mb], 49152\n\t"
    "s_nop 0\n\t"
    "buffer_load_dwordx4 v0, %[rx], s46 offen lds\n\t"
    "s_add_u32 m0, %[mb], 50176\n\t"
    "s_nop 0\n\t"
    "buffer_load_dwordx4 v1, %[rx], s46 offen lds\n\t"
    "s_add_u32 m0, %[mb], 51200\n\t"
    "s_nop 0\n\t"
    "buffer_load_dwordx4 v2, %[rx], s46 offen lds\n\t"
    "s_add_u32 m0, %[mb], 52224\n\t"
    "s_nop 0\n\t"
    "buffer_load_dwordx4 v3, %[rx], s46 offen lds\n\t"
    "s_add_u32 s46, s41, 512\n\t"
    "s_add_u32 m0, %[mb], 65536\n\t"
    "s_nop 0\n\t"
    "buffer_load_dwordx4 v0, %[rx], s46 offen lds\n\t"
    "s_add_u32 m0, %[mb], 66560\n\t"
    "s_nop 0\n\t"
    "buffer_load_dwordx4 v1, %[rx], s46 offen lds\n\t"
    "s_add_u32 m0, %[mb], 67584\n\t"
    "s_nop 0\n\t"
    "buffer_load_dwordx4 v2, %[rx], s46 offen lds\n\t"
    "s_add_u32 m0, %[mb], 68608\n\t"
    "s_nop 0\n\t"
    "buffer_load_dwordx4 v3, %[rx], s46 offen lds\n\t"
    "s_add_u32 s46, s41, 640\n\t"
    "s_add_u32 m0, %[mb], 81920\n\t"
    "s_nop 0\n\t"
    "buffer_load_dwordx4 v0, %[rx], s46 offen lds\n\t"
    "s_add_u32 m0, %[mb], 82944\n\t"
    "s_nop 0\n\t"
    "buffer_load_dwordx4 v1, %[rx], s46 offen lds\n\t"
    "s_add_u32 m0, %[mb], 83968\n\t"
    "s_nop 0\n\t"
    "buffer_load_dwordx4 v2, %[rx], s46 offen lds\n\t"
    "s_add_u32 m0, %[mb], 84992\n\t"
    "s_nop 0\n\t"
    "buffer_load_dwordx4 v3, %[rx], s46 offen lds\n\t"
    "s_add_u32 s46, s41, 768\n\t"
    "s_add_u32 m0, %[mb], 98304\n\t"
    "s_nop 0\n\t"
    "buffer_load_dwordx4 v0, %[rx], s46 offen lds\n\t"
    "s_add_u32 m0, %[mb], 99328\n\t"
    "s_nop 0\n\t"
    "buffer_load_dwordx4 v1, %[rx], s46 offen lds\n\t"
    "s_add_u32 m0, %[mb], 100352\n\t"
    "s_nop 0\n\t"
    "buffer_load_dwordx4 v2, %[rx], s46 offen lds\n\t"
    "s_add_u32 m0, %[mb], 101376\n\t"
    "s_nop 0\n\t"
    "buffer_load_dwordx4 v3, %[rx], s46 offen lds\n\t"
    "s_add_u32 s46, s41, 896\n\t"
    "s_add_u32 m0, %[mb], 114688\n\t"
    "s_nop 0\n\t"
    "buffer_load_dwordx4 v0, %[rx], s46 offen lds\n\t"
    "s_add_u32 m0, %[mb], 115712\n\t"
    "s_nop 0\n\t"
    "buffer_load_dwordx4 v1, %[rx], s46 offen lds\n\t"
    "s_add_u32 m0, %[mb], 116736\n\t"
    "s_nop 0\n\t"
    "buffer_load_dwordx4 v2, %[rx], s46 offen lds\n\t"
    "s_add_u32 m0, %[mb], 117760\n\t"
    "s_nop 0\n\t"
    "buffer_load_dwordx4 v3, %[rx], s46 offen lds\n\t"
    "s_mov_b32 s46, %[sg]\n\t"
    "4:\n\t"
    "s_cmp_eq_u32 s46, 0\n\t"
    "s_cbranch_scc1 5f\n\t"
    "s_sleep 6\n\t"
    "s_sub_u32 s46, s46, 1\n\t"
    "s_branch 4b\n\t"
    "5:\n\t"
    "s_waitcnt vmcnt(28)\n\t"
    "s_barrier\n\t"
    "ds_read_b128 v[32:35], v4 offset:0\n\t"
    "ds_read_b128 v[36:39], v4 offset:2048\n\t"
    "ds_read_b128 v[40:43], v4 offset:4096\n\t"
    "ds_read_b128 v[44:47], v4 offset:6144\n\t"
    "ds_read_b128 v[48:51], v4 offset:8192\n\t"
    "ds_read_b128 v[52:55], v4 offset:10240\n\t"
    "ds_read_b128 v[56:59], v4 offset:12288\n\t"
    "ds_read_b128 v[60:63], v4 offset:14336\n\t"
    "1:\n\t"
    "s_add_u32 s41, s41, %[xt]\n\t"
    "s_cmp_eq_u32 s40, 1\n\t"
    "s_cselect_b32 s41, 0x40000000, s41\n\t"
    "s_waitcnt lgkmcnt(7)\n\t"
    "v_mfma_f32_16x16x32_bf16 v[128:131], a[0:3], v[32:35], 0\n\t"
    "ds_read_b128 v[64:67], v6 offset:0\n\t"
    "v_mfma_f32_16x16x32_bf16 v[132:135], a[4:7], v[32:35], 0\n\t"
    "v_mfma_f32_16x16x32_bf16 v[136:139], a[8:11], v[32:35], 0\n\t"
    "ds_read_b128 v[68:71], v6 offset:2048\n\t"
    "v_mfma_f32_16x16x32_bf16 v[140:143], a[12:15], v[32:35], 0\n\t"
    "s_waitcnt lgkmcnt(8)\n\t"
    "v_mfma_f32_16x16x32_bf16 v[144:147], a[0:3], v[36:39], 0\n\t"
    "ds_read_b128 v[72:75], v6 offset:4096\n\t"
    "v_mfma_f32_16x16x32_bf16 v[148:151], a[4:7], v[36:39], 0\n\t"
    "v_mfma_f32_16x16x32_bf16 v[152:155], a[8:11], v[36:39], 0\n\t"
    "ds_read_b128 v[76:79], v6 offset:6144\n\t"
    "v_mfma_f32_16x16x32_bf16 v[156:159], a[12:15], v[36:39], 0\n\t"
    "s_waitcnt lgkmcnt(9)\n\t"
    "v_mfma_f32_16x16x32_bf16 v[160:163], a[0:3], v[40:43], 0\n\t"
    "ds_read_b128 v[80:83], v6 offset:8192\n\t"
    "v_mfma_f32_16x16x32_bf16 v[164:167], a[4:7], v[40:43], 0\n\t"
    "v_mfma_f32_16x16x32_bf16 v[168:171], a[8:11], v[40:43], 0\n\t"
    "ds_read_b128 v[84:87], v6 offset:10240\n\t"
    "v_mfma_f32_16x16x32_bf16 v[172:175], a[12:15], v[40:43], 0\n\t"
    "s_waitcnt lgkmcnt(10)\n\t"
    "v_mfma_f32_16x16x32_bf16 v[176:179], a[0:3], v[44:47], 0\n\t"
    "ds_read_b128 v[88:91], v6 offset:12288\n\t"
    "v_mfma_f32_16x16x32_bf16 v[180:183], a[4:7], v[44:47], 0\n\t"
    "v_mfma_f32_16x16x32_bf16 v[184:187], a[8:11], v[44:47], 0\n\t"
    "ds_read_b128 v[92:95], v6 offset:14336\n\t"
    "v_mfma_f32_16x16x32_bf16 v[188:191], a[12:15], v[44:47], 0\n\t"
    "s_waitcnt lgkmcnt(11)\n\t"
    "v_mfma_f32_16x16x32_bf16 v[192:195], a[0:3], v[48:51], 0\n\t"
    "v_mfma_f32_16x16x32_bf16 v[196:199], a[4:7], v[48:51], 0\n\t"
    "v_mfma_f32_16x16x32_bf16 v[200:203], a[8:11], v[48:51], 0\n\t"
    "v_mfma_f32_16x16x32_bf16 v[204:207], a[12:15], v[48:51], 0\n\t"
    "s_waitcnt lgkmcnt(10)\n\t"
    "v_mfma_f32_16x16x32_bf16 v[208:211], a[0:3], v[52:55], 0\n\t"
    "v_mfma_f32_16x16x32_bf16 v[212:215], a[4:7], v[52:55], 0\n\t"
    "v_mfma_f32_16x16x32_bf16 v[216:219], a[8:11], v[52:55], 0\n\t"
    "v_mfma_f32_16x16x32_bf16 v[220:223], a[12:15], v[52:55], 0\n\t"
    "s_waitcnt lgkmcnt(9)\n\t"
    "v_mfma_f32_16x16x32_bf16 v[224:227], a[0:3], v[56:59], 0\n\t"
    "v_mfma_f32_16x16x32_bf16 v[228:231], a[4:7], v[56:59], 0\n\t"
    "v_mfma_f32_16x16x32_bf16 v[232:235], a[8:11], v[56:59], 0\n\t"
    "v_mfma_f32_16x16x32_bf16 v[236:239], a[12:15], v[56:59], 0\n\t"
    "s_waitcnt lgkmcnt(8)\n\t"
    "v_mfma_f32_16x16x32_bf16 v[240:243], a[0:3], v[60:63], 0\n\t"
    "v_mfma_f32_16x16x32_bf16 v[244:247], a[4:7], v[60:63], 0\n\t"
    "v_mfma_f32_16x16x32_bf16 v[248:251], a[8:11], v[60:63], 0\n\t"
    "v_mfma_f32_16x16x32_bf16 v[252:255], a[12:15], v[60:63], 0\n\t"
    "s_waitcnt vmcnt(0)\n\t"
    "s_waitcnt lgkmcnt(0)\n\t"
    "s_barrier\n\t"
    "s_cmp_lg_u32 %[w], 0\n\t"
    "s_cbranch_scc1 2f\n\t"
    "s_mov_b64 exec, 1\n\t"
    "buffer_store_dword v9, off, %[rc], s44 sc1\n\t"
    "s_mov_b64 exec, -1\n\t"
    "2:\n\t"
    "v_mfma_f32_16x16x32_bf16 v[128:131], a[16:19], v[64:67], v[128:131]\n\t"
    "ds_read_b128 v[32:35], v4 offset:16384\n\t"
    "v_mfma_f32_16x16x32_bf16 v[132:135], a[20:23], v[64:67], v[132:135]\n\t"
    "v_mfma_f32_16x16x32_bf16 v[136:139], a[24:27], v[64:67], v[136:139]\n\t"
    "ds_read_b128 v[36:39], v4 offset:18432\n\t"
    "v_mfma_f32_16x16x32_bf16 v[140:143], a[28:31], v[64:67], v[140:143]\n\t"
    "s_add_u32 s46, s41, 0\n\t"
    "s_add_u32 m0, %[mb], 0\n\t"
    "s_nop 0\n\t"
    "buffer_load_dwordx4 v0, %[rx], s46 offen lds\n\t"
    "v_mfma_f32_16x16x32_bf16 v[144:147], a[16:19], v[68:71], v[144:147]\n\t"
    "ds_read_b128 v[40:43], v4 offset:20480\n\t"
    "v_mfma_f32_16x16x32_bf16 v[148:151], a[20:23], v[68:71], v[148:151]\n\t"
    "v_mfma_f32_16x16x32_bf16 v[152:155], a[24:27], v[68:71], v[152:155]\n\t"
    "ds_read_b128 v[44:47], v4 offset:22528\n\t"
    "v_mfma_f32_16x16x32_bf16 v[156:159], a[28:31], v[68:71], v[156:159]\n\t"
    "v_mfma_f32_16x16x32_bf16 v[160:163], a[16:19], v[72:75], v[160:163]\n\t"
    "ds_read_b128 v[48:51], v4 offset:24576\n\t"
    "v_mfma_f32_16x16x32_bf16 v[164:167], a[20:23], v[72:75], v[164:167]\n\t"
    "s_add_u32 m0, %[mb], 1024\n\t"
    "s_nop 0\n\t"
    "buffer_load_dwordx4 v1, %[rx], s46 offen lds\n\t"
    "v_mfma_f32_16x16x32_bf16 v[168:171], a[24:27], v[72:75], v[168:171]\n\t"
    "ds_read_b128 v[52:55], v4 offset:26624\n\t"
    "v_mfma_f32_16x16x32_bf16 v[172:175], a[28:31], v[72:75], v[172:175]\n\t"
    "v_mfma_f32_16x16x32_bf16 v[176:179], a[16:19], v[76:79], v[176:179]\n\t"
    "ds_read_b128 v[56:59], v4 offset:28672\n\t"
    "v_mfma_f32_16x16x32_bf16 v[180:183], a[20:23], v[76:79], v[180:183]\n\t"
    "v_mfma_f32_16x16x32_bf16 v[184:187], a[24:27], v[76:79], v[184:187]\n\t"
    "ds_read_b128 v[60:63], v4 offset:30720\n\t"
    "v_mfma_f32_16x16x32_bf16 v[188:191], a[28:31], v[76:79], v[188:191]\n\t"
    "s_add_u32 m0, %[mb], 2048\n\t"
    "s_nop 0\n\t"
    "buffer_load_dwordx4 v2, %[rx], s46 offen lds\n\t"
    "v_mfma_f32_16x16x32_bf16 v[192:195], a[16:19], v[80:83], v[192:195]\n\t"
    "v_mfma_f32_16x16x32_bf16 v[196:199], a[20:23], v[80:83], v[196:199]\n\t"
    "v_mfma_f32_16x16x32_bf16 v[200:203], a[24:27], v[80:83], v[200:203]\n\t"
    "v_mfma_f32_16x16x32_bf16 v[204:207], a[28:31], v[80:83], v[204:207]\n\t"
    "v_mfma_f32_16x16x32_bf16 v[208:211], a[16:19], v[84:87], v[208:211]\n\t"
    "v_mfma_f32_16x16x32_bf16 v[212:215], a[20:23], v[84:87], v[212:215]\n\t"
    "s_add_u32 m0, %[mb], 3072\n\t"
    "s_nop 0\n\t"
    "buffer_load_dwordx4 v3, %[rx], s46 offen lds\n\t"
    "v_mfma_f32_16x16x32_bf16 v[216:219], a[24:27], v[84:87], v[216:219]\n\t"
    "v_mfma_f32_16x16x32_bf16 v[220:223], a[28:31], v[84:87], v[220:223]\n\t"
    "v_mfma_f32_16x16x32_bf16 v[224:227], a[16:19], v[88:91], v[224:227]\n\t"
    "v_mfma_f32_16x16x32_bf16 v[228:231], a[20:23], v[88:91], v[228:231]\n\t"
    "v_mfma_f32_16x16x32_bf16 v[232:235], a[24:27], v[88:91], v[232:235]\n\t"
    "v_mfma_f32_16x16x32_bf16 v[236:239], a[28:31], v[88:91], v[236:239]\n\t"
    "v_mfma_f32_16x16x32_bf16 v[240:243], a[16:19], v[92:95], v[240:243]\n\t"
    "v_mfma_f32_16x16x32_bf16 v[244:247], a[20:23], v[92:95], v[244:247]\n\t"
    "v_mfma_f32_16x16x32_bf16 v[248:251], a[24:27], v[92:95], v[248:251]\n\t"
    "v_mfma_f32_16x16x32_bf16 v[252:255], a[28:31], v[92:95], v[252:255]\n\t"
    "s_waitcnt lgkmcnt(7)\n\t"
    "v_mfma_f32_16x16x32_bf16 v[128:131], a[32:35], v[32:35], v[128:131]\n\t"
    "ds_read_b128 v[64:67], v6 offset:16384\n\t"
    "v_mfma_f32_16x16x32_bf16 v[132:135], a[36:39], v[32:35], v[132:135]\n\t"
    "v_mfma_f32_16x16x32_bf16 v[136:139], a[40:43], v[32:35], v[136:139]\n\t"
    "ds_read_b128 v[68:71], v6 offset:18432\n\t"
    "v_mfma_f32_16x16x32_bf16 v[140:143], a[44:47], v[32:35], v[140:143]\n\t"
    "s_waitcnt lgkmcnt(8)\n\t"
    "v_mfma_f32_16x16x32_bf16 v[144:147], a[32:35], v[36:39], v[144:147]\n\t"
    "ds_read_b128 v[72:75], v6 offset:20480\n\t"
    "v_mfma_f32_16x16x32_bf16 v[148:151], a[36:39], v[36:39], v[148:151]\n\t"
    "v_mfma_f32_16x16x32_bf16 v[152:155], a[40:43], v[36:39], v[152:155]\n\t"
    "ds_read_b128 v[76:79], v6 offset:22528\n\t"
    "v_mfma_f32_16x16x32_bf16 v[156:159], a[44:47], v[36:39], v[156:159]\n\t"
    "s_waitcnt lgkmcnt(9)\n\t"
    "v_mfma_f32_16x16x32_bf16 v[160:163], a[32:35], v[40:43], v[160:163]\n\t"
    "ds_read_b128 v[80:83], v6 offset:24576\n\t"
    "v_mfma_f32_16x16x32_bf16 v[164:167], a[36:39], v[40:43], v[164:167]\n\t"
    "v_mfma_f32_16x16x32_bf16 v[168:171], a[40:43], v[40:43], v[168:171]\n\t"
    "ds_read_b128 v[84:87], v6 offset:26624\n\t"
    "v_mfma_f32_16x16x32_bf16 v[172:175], a[44:47], v[40:43], v[172:175]\n\t"
    "s_waitcnt lgkmcnt(10)\n\t"
    "v_mfma_f32_16x16x32_bf16 v[176:179], a[32:35], v[44:47], v[176:179]\n\t"
    "ds_read_b128 v[88:91], v6 offset:28672\n\t"
    "v_mfma_f32_16x16x32_bf16 v[180:183], a[36:39], v[44:47], v[180:183]\n\t"
    "v_mfma_f32_16x16x32_bf16 v[184:187], a[40:43], v[44:47], v[184:187]\n\t"
    "ds_read_b128 v[92:95], v6 offset:30720\n\t"
    "v_mfma_f32_16x16x32_bf16 v[188:191], a[44:47], v[44:47], v[188:191]\n\t"
    "s_waitcnt lgkmcnt(11)\n\t"
    "v_mfma_f32_16x16x32_bf16 v[192:195], a[32:35], v[48:51], v[192:195]\n\t"
    "v_mfma_f32_16x16x32_bf16 v[196:199], a[36:39], v[48:51], v[196:199]\n\t"
    "v_mfma_f32_16x16x32_bf16 v[200:203], a[40:43], v[48:51], v[200:203]\n\t"
    "v_mfma_f32_16x16x32_bf16 v[204:207], a[44:47], v[48:51], v[204:207]\n\t"
    "s_waitcnt lgkmcnt(10)\n\t"
    "v_mfma_f32_16x16x32_bf16 v[208:211], a[32:35], v[52:55], v[208:211]\n\t"
    "v_mfma_f32_16x16x32_bf16 v[212:215], a[36:39], v[52:55], v[212:215]\n\t"
    "v_mfma_f32_16x16x32_bf16 v[216:219], a[40:43], v[52:55], v[216:219]\n\t"
    "v_mfma_f32_16x16x32_bf16 v[220:223], a[44:47], v[52:55], v[220:223]\n\t"
    "s_waitcnt lgkmcnt(9)\n\t"
    "v_mfma_f32_16x16x32_bf16 v[224:227], a[32:35], v[56:59], v[224:227]\n\t"
    "v_mfma_f32_16x16x32_bf16 v[228:231], a[36:39], v[56:59], v[228:231]\n\t"
    "v_mfma_f32_16x16x32_bf16 v[232:235], a[40:43], v[56:59], v[232:235]\n\t"
    "v_mfma_f32_16x16x32_bf16 v[236:239], a[44:47], v[56:59], v[236:239]\n\t"
    "s_waitcnt lgkmcnt(8)\n\t"
    "v_mfma_f32_16x16x32_bf16 v[240:243], a[32:35], v[60:63], v[240:243]\n\t"
    "v_mfma_f32_16x16x32_bf16 v[244:247], a[36:39], v[60:63], v[244:247]\n\t"
    "v_mfma_f32_16x16x32_bf16 v[248:251], a[40:43], v[60:63], v[248:251]\n\t"
    "v_mfma_f32_16x16x32_bf16 v[252:255], a[44:47], v[60:63], v[252:255]\n\t"
    "s_waitcnt vmcnt(24)\n\t"
    "s_waitcnt lgkmcnt(0)\n\t"
    "s_barrier\n\t"
    "v_mfma_f32_16x16x32_bf16 v[128:131], a[48:51], v[64:67], v[128:131]\n\t"
    "ds_read_b128 v[32:35], v4 offset:32768\n\t"
    "v_mfma_f32_16x16x32_bf16 v[132:135], a[52:55], v[64:67], v[132:135]\n\t"
    "v_mfma_f32_16x16x32_bf16 v[136:139], a[56:59], v[64:67], v[136:139]\n\t"
    "ds_read_b128 v[36:39], v4 offset:34816\n\t"
    "v_mfma_f32_16x16x32_bf16 v[140:143], a[60:63], v[64:67], v[140:143]\n\t"
    "s_add_u32 s46, s41, 128\n\t"
    "s_add_u32 m0, %[mb], 16384\n\t"
    "s_nop 0\n\t"
    "buffer_load_dwordx4 v0, %[rx], s46 offen lds\n\t"
    "v_mfma_f32_16x16x32_bf16 v[144:147], a[48:51], v[68:71], v[144:147]\n\t"
    "ds_read_b128 v[40:43], v4 offset:36864\n\t"
    "v_mfma_f32_16x16x32_bf16 v[148:151], a[52:55], v[68:71], v[148:151]\n\t"
    "v_mfma_f32_16x16x32_bf16 v[152:155], a[56:59], v[68:71], v[152:155]\n\t"
    "ds_read_b128 v[44:47], v4 offset:38912\n\t"
    "v_mfma_f32_16x16x32_bf16 v[156:159], a[60:63], v[68:71], v[156:159]\n\t"
    "v_mfma_f32_16x16x32_bf16 v[160:163], a[48:51], v[72:75], v[160:163]\n\t"
    "ds_read_b128 v[48:51], v4 offset:40960\n\t"
    "v_mfma_f32_16x16x32_bf16 v[164:167], a[52:55], v[72:75], v[164:167]\n\t"
    "s_add_u32 m0, %[mb], 17408\n\t"
    "s_nop 0\n\t"
    "buffer_load_dwordx4 v1, %[rx], s46 offen lds\n\t"
    "v_mfma_f32_16x16x32_bf16 v[168:171], a[56:59], v[72:75], v[168:171]\n\t"
    "ds_read_b128 v[52:55], v4 offset:43008\n\t"
    "v_mfma_f32_16x16x32_bf16 v[172:175], a[60:63], v[72:75], v[172:175]\n\t"
    "v_mfma_f32_16x16x32_bf16 v[176:179], a[48:51], v[76:79], v[176:179]\n\t"
    "ds_read_b128 v[56:59], v4 offset:45056\n\t"
    "v_mfma_f32_16x16x32_bf16 v[180:183], a[52:55], v[76:79], v[180:183]\n\t"
    "v_mfma_f32_16x16x32_bf16 v[184:187], a[56:59], v[76:79], v[184:187]\n\t"
    "ds_read_b128 v[60:63], v4 offset:47104\n\t"
    "v_mfma_f32_16x16x32_bf16 v[188:191], a[60:63], v[76:79], v[188:191]\n\t"
    "s_add_u32 m0, %[mb], 18432\n\t"
    "s_nop 0\n\t"
    "buffer_load_dwordx4 v2, %[rx], s46 offen lds\n\t"
    "v_mfma_f32_16x16x32_bf16 v[192:195], a[48:51], v[80:83], v[192:195]\n\t"
    "v_mfma_f32_16x16x32_bf16 v[196:199], a[52:55], v[80:83], v[196:199]\n\t"
    "v_mfma_f32_16x16x32_bf16 v[200:203], a[56:59], v[80:83], v[200:203]\n\t"
    "v_mfma_f32_16x16x32_bf16 v[204:207], a[60:63], v[80:83], v[204:207]\n\t"
    "v_mfma_f32_16x16x32_bf16 v[208:211], a[48:51], v[84:87], v[208:211]\n\t"
    "v_mfma_f32_16x16x32_bf16 v[212:215], a[52:55], v[84:87], v[212:215]\n\t"
    "s_add_u32 m0, %[mb], 19456\n\t"
    "s_nop 0\n\t"
    "buffer_load_dwordx4 v3, %[rx], s46 offen lds\n\t"
    "v_mfma_f32_16x16x32_bf16 v[216:219], a[56:59], v[84:87], v[216:219]\n\t"
    "v_mfma_f32_16x16x32_bf16 v[220:223], a[60:63], v[84:87], v[220:223]\n\t"
    "v_mfma_f32_16x16x32_bf16 v[224:227], a[48:51], v[88:91], v[224:227]\n\t"
    "v_mfma_f32_16x16x32_bf16 v[228:231], a[52:55], v[88:91], v[228:231]\n\t"
    "v_mfma_f32_16x16x32_bf16 v[232:235], a[56:59], v[88:91], v[232:235]\n\t"
    "v_mfma_f32_16x16x32_bf16 v[236:239], a[60:63], v[88:91], v[236:239]\n\t"
    "v_mfma_f32_16x16x32_bf16 v[240:243], a[48:51], v[92:95], v[240:243]\n\t"
    "v_mfma_f32_16x16x32_bf16 v[244:247], a[52:55], v[92:95], v[244:247]\n\t"
    "v_mfma_f32_16x16x32_bf16 v[248:251], a[56:59], v[92:95], v[248:251]\n\t"
    "v_mfma_f32_16x16x32_bf16 v[252:255], a[60:63], v[92:95], v[252:255]\n\t"
    "s_waitcnt lgkmcnt(7)\n\t"
    "v_mfma_f32_16x16x32_bf16 v[128:131], a[64:67], v[32:35], v[128:131]\n\t"
    "ds_read_b128 v[64:67], v6 offset:32768\n\t"
    "v_mfma_f32_16x16x32_bf16 v[132:135], a[68:71], v[32:35], v[132:135]\n\t"
    "v_mfma_f32_16x16x32_bf16 v[136:139], a[72:75], v[32:35], v[136:139]\n\t"
    "ds_read_b128 v[68:71], v6 offset:34816\n\t"
    "v_mfma_f32_16x16x32_bf16 v[140:143], a[76:79], v[32:35], v[140:143]\n\t"
    "s_waitcnt lgkmcnt(8)\n\t"
    "v_mfma_f32_16x16x32_bf16 v[144:147], a[64:67], v[36:39], v[144:147]\n\t"
    "ds_read_b128 v[72:75], v6 offset:36864\n\t"
    "v_mfma_f32_16x16x32_bf16 v[148:151], a[68:71], v[36:39], v[148:151]\n\t"
    "v_mfma_f32_16x16x32_bf16 v[152:155], a[72:75], v[36:39], v[152:155]\n\t"
    "ds_read_b128 v[76:79], v6 offset:38912\n\t"
    "v_mfma_f32_16x16x32_bf16 v[156:159], a[76:79], v[36:39], v[156:159]\n\t"
    "s_waitcnt lgkmcnt(9)\n\t"
    "v_mfma_f32_16x16x32_bf16 v[160:163], a[64:67], v[40:43], v[160:163]\n\t"
    "ds_read_b128 v[80:83], v6 offset:40960\n\t"
    "v_mfma_f32_16x16x32_bf16 v[164:167], a[68:71], v[40:43], v[164:167]\n\t"
    "v_mfma_f32_16x16x32_bf16 v[168:171], a[72:75], v[40:43], v[168:171]\n\t"
    "ds_read_b128 v[84:87], v6 offset:43008\n\t"
    "v_mfma_f32_16x16x32_bf16 v[172:175], a[76:79], v[40:43], v[172:175]\n\t"
    "s_waitcnt lgkmcnt(10)\n\t"
    "v_mfma_f32_16x16x32_bf16 v[176:179], a[64:67], v[44:47], v[176:179]\n\t"
    "ds_read_b128 v[88:91], v6 offset:45056\n\t"
    "v_mfma_f32_16x16x32_bf16 v[180:183], a[68:71], v[44:47], v[180:183]\n\t"
    "v_mfma_f32_16x16x32_bf16 v[184:187], a[72:75], v[44:47], v[184:187]\n\t"
    "ds_read_b128 v[92:95], v6 offset:47104\n\t"
    "v_mfma_f32_16x16x32_bf16 v[188:191], a[76:79], v[44:47], v[188:191]\n\t"
    "s_waitcnt lgkmcnt(11)\n\t"
    "v_mfma_f32_16x16x32_bf16 v[192:195], a[64:67], v[48:51], v[192:195]\n\t"
    "v_mfma_f32_16x16x32_bf16 v[196:199], a[68:71], v[48:51], v[196:199]\n\t"
    "v_mfma_f32_16x16x32_bf16 v[200:203], a[72:75], v[48:51], v[200:203]\n\t"
    "v_mfma_f32_16x16x32_bf16 v[204:207], a[76:79], v[48:51], v[204:207]\n\t"
    "s_waitcnt lgkmcnt(10)\n\t"
    "v_mfma_f32_16x16x32_bf16 v[208:211], a[64:67], v[52:55], v[208:211]\n\t"
    "v_mfma_f32_16x16x32_bf16 v[212:215], a[68:71], v[52:55], v[212:215]\n\t"
    "v_mfma_f32_16x16x32_bf16 v[216:219], a[72:75], v[52:55], v[216:219]\n\t"
    "v_mfma_f32_16x16x32_bf16 v[220:223], a[76:79], v[52:55], v[220:223]\n\t"
    "s_waitcnt lgkmcnt(9)\n\t"
    "v_mfma_f32_16x16x32_bf16 v[224:227], a[64:67], v[56:59], v[224:227]\n\t"
    "v_mfma_f32_16x16x32_bf16 v[228:231], a[68:71], v[56:59], v[228:231]\n\t"
    "v_mfma_f32_16x16x32_bf16 v[232:235], a[72:75], v[56:59], v[232:235]\n\t"
    "v_mfma_f32_16x16x32_bf16 v[236:239], a[76:79], v[56:59], v[236:239]\n\t"
    "s_waitcnt lgkmcnt(8)\n\t"
    "v_mfma_f32_16x16x32_bf16 v[240:243], a[64:67], v[60:63], v[240:243]\n\t"
    "v_mfma_f32_16x16x32_bf16 v[244:247], a[68:71], v[60:63], v[244:247]\n\t"
    "v_mfma_f32_16x16x32_bf16 v[248:251], a[72:75], v[60:63], v[248:251]\n\t"
    "v_mfma_f32_16x16x32_bf16 v[252:255], a[76:79], v[60:63], v[252:255]\n\t"
    "s_waitcnt vmcnt(24)\n\t"
    "s_waitcnt lgkmcnt(0)\n\t"
    "s_barrier\n\t"
    "v_mfma_f32_16x16x32_bf16 v[128:131], a[80:83], v[64:67], v[128:131]\n\t"
    "ds_read_b128 v[32:35], v4 offset:49152\n\t"
    "v_mfma_f32_16x16x32_bf16 v[132:135], a[84:87], v[64:67], v[132:135]\n\t"
    "v_mfma_f32_16x16x32_bf16 v[136:139], a[88:91], v[64:67], v[136:139]\n\t"
    "ds_read_b128 v[36:39], v4 offset:51200\n\t"
    "v_mfma_f32_16x16x32_bf16 v[140:143], a[92:95], v[64:67], v[140:143]\n\t"
    "s_add_u32 s46, s41, 256\n\t"
    "s_add_u32 m0, %[mb], 32768\n\t"
    "s_nop 0\n\t"
    "buffer_load_dwordx4 v0, %[rx], s46 offen lds\n\t"
    "v_mfma_f32_16x16x32_bf16 v[144:147], a[80:83], v[68:71], v[144:147]\n\t"
    "ds_read_b128 v[40:43], v4 offset:53248\n\t"
    "v_mfma_f32_16x16x32_bf16 v[148:151], a[84:87], v[68:71], v[148:151]\n\t"
    "v_mfma_f32_16x16x32_bf16 v[152:155], a[88:91], v[68:71], v[152:155]\n\t"
    "ds_read_b128 v[44:47], v4 offset:55296\n\t"
    "v_mfma_f32_16x16x32_bf16 v[156:159], a[92:95], v[68:71], v[156:159]\n\t"
    "v_mfma_f32_16x16x32_bf16 v[160:163], a[80:83], v[72:75], v[160:163]\n\t"
    "ds_read_b128 v[48:51], v4 offset:57344\n\t"
    "v_mfma_f32_16x16x32_bf16 v[164:167], a[84:87], v[72:75], v[164:167]\n\t"
    "s_add_u32 m0, %[mb], 33792\n\t"
    "s_nop 0\n\t"
    "buffer_load_dwordx4 v1, %[rx], s46 offen lds\n\t"
    "v_mfma_f32_16x16x32_bf16 v[168:171], a[88:91], v[72:75], v[168:171]\n\t"
    "ds_read_b128 v[52:55], v4 offset:59392\n\t"
    "v_mfma_f32_16x16x32_bf16 v[172:175], a[92:95], v[72:75], v[172:175]\n\t"
    "v_mfma_f32_16x16x32_bf16 v[176:179], a[80:83], v[76:79], v[176:179]\n\t"
    "ds_read_b128 v[56:59], v4 offset:61440\n\t"
    "v_mfma_f32_16x16x32_bf16 v[180:183], a[84:87], v[76:79], v[180:183]\n\t"
    "v_mfma_f32_16x16x32_bf16 v[184:187], a[88:91], v[76:79], v[184:187]\n\t"
    "ds_read_b128 v[60:63], v4 offset:63488\n\t"
    "v_mfma_f32_16x16x32_bf16 v[188:191], a[92:95], v[76:79], v[188:191]\n\t"
    "s_add_u32 m0, %[mb], 34816\n\t"
    "s_nop 0\n\t"
    "buffer_load_dwordx4 v2, %[rx], s46 offen lds\n\t"
    "v_mfma_f32_16x16x32_bf16 v[192:195], a[80:83], v[80:83], v[192:195]\n\t"
    "v_mfma_f32_16x16x32_bf16 v[196:199], a[84:87], v[80:83], v[196:199]\n\t"
    "v_mfma_f32_16x16x32_bf16 v[200:203], a[88:91], v[80:83], v[200:203]\n\t"
    "v_mfma_f32_16x16x32_bf16 v[204:207], a[92:95], v[80:83], v[204:207]\n\t"
    "v_mfma_f32_16x16x32_bf16 v[208:211], a[80:83], v[84:87], v[208:211]\n\t"
    "v_mfma_f32_16x16x32_bf16 v[212:215], a[84:87], v[84:87], v[212:215]\n\t"
    "s_add_u32 m0, %[mb], 35840\n\t"
    "s_nop 0\n\t"
    "buffer_load_dwordx4 v3, %[rx], s46 offen lds\n\t"
    "v_mfma_f32_16x16x32_bf16 v[216:219], a[88:91], v[84:87], v[216:219]\n\t"
    "v_mfma_f32_16x16x32_bf16 v[220:223], a[92:95], v[84:87], v[220:223]\n\t"
    "v_mfma_f32_16x16x32_bf16 v[224:227], a[80:83], v[88:91], v[224:227]\n\t"
    "v_mfma_f32_16x16x32_bf16 v[228:231], a[84:87], v[88:91], v[228:231]\n\t"
    "v_mfma_f32_16x16x32_bf16 v[232:235], a[88:91], v[88:91], v[232:235]\n\t"
    "v_mfma_f32_16x16x32_bf16 v[236:239], a[92:95], v[88:91], v[236:239]\n\t"
    "v_mfma_f32_16x16x32_bf16 v[240:243], a[80:83], v[92:95], v[240:243]\n\t"
    "v_mfma_f32_16x16x32_bf16 v[244:247], a[84:87], v[92:95], v[244:247]\n\t"
    "v_mfma_f32_16x16x32_bf16 v[248:251], a[88:91], v[92:95], v[248:251]\n\t"
    "v_mfma_f32_16x16x32_bf16 v[252:255], a[92:95], v[92:95], v[252:255]\n\t"
    "s_waitcnt lgkmcnt(7)\n\t"
    "v_mfma_f32_16x16x32_bf16 v[128:131], a[96:99], v[32:35], v[128:131]\n\t"
    "ds_read_b128 v[64:67], v6 offset:49152\n\t"
    "v_mfma_f32_16x16x32_bf16 v[132:135], a[100:103], v[32:35], v[132:135]\n\t"
    "v_mfma_f32_16x16x32_bf16 v[136:139], a[104:107], v[32:35], v[136:139]\n\t"
    "ds_read_b128 v[68:71], v6 offset:51200\n\t"
    "v_mfma_f32_16x16x32_bf16 v[140:143], a[108:111], v[32:35], v[140:143]\n\t"
    "s_waitcnt lgkmcnt(8)\n\t"
    "v_mfma_f32_16x16x32_bf16 v[144:147], a[96:99], v[36:39], v[144:147]\n\t"
    "ds_read_b128 v[72:75], v6 offset:53248\n\t"
    "v_mfma_f32_16x16x32_bf16 v[148:151], a[100:103], v[36:39], v[148:151]\n\t"
    "v_mfma_f32_16x16x32_bf16 v[152:155], a[104:107], v[36:39], v[152:155]\n\t"
    "ds_read_b128 v[76:79], v6 offset:55296\n\t"
    "v_mfma_f32_16x16x32_bf16 v[156:159], a[108:111], v[36:39], v[156:159]\n\t"
    "s_waitcnt lgkmcnt(9)\n\t"
    "v_mfma_f32_16x16x32_bf16 v[160:163], a[96:99], v[40:43], v[160:163]\n\t"
    "ds_read_b128 v[80:83], v6 offset:57344\n\t"
    "v_mfma_f32_16x16x32_bf16 v[164:167], a[100:103], v[40:43], v[164:167]\n\t"
    "v_mfma_f32_16x16x32_bf16 v[168:171], a[104:107], v[40:43], v[168:171]\n\t"
    "ds_read_b128 v[84:87], v6 offset:59392\n\t"
    "v_mfma_f32_16x16x32_bf16 v[172:175], a[108:111], v[40:43], v[172:175]\n\t"
    "s_waitcnt lgkmcnt(10)\n\t"
    "v_mfma_f32_16x16x32_bf16 v[176:179], a[96:99], v[44:47], v[176:179]\n\t"
    "ds_read_b128 v[88:91], v6 offset:61440\n\t"
    "v_mfma_f32_16x16x32_bf16 v[180:183], a[100:103], v[44:47], v[180:183]\n\t"
    "v_mfma_f32_16x16x32_bf16 v[184:187], a[104:107], v[44:47], v[184:187]\n\t"
    "ds_read_b128 v[92:95], v6 offset:63488\n\t"
    "v_mfma_f32_16x16x32_bf16 v[188:191], a[108:111], v[44:47], v[188:191]\n\t"
    "s_waitcnt lgkmcnt(11)\n\t"
    "v_mfma_f32_16x16x32_bf16 v[192:195], a[96:99], v[48:51], v[192:195]\n\t"
    "v_mfma_f32_16x16x32_bf16 v[196:199], a[100:103], v[48:51], v[196:199]\n\t"
    "v_mfma_f32_16x16x32_bf16 v[200:203], a[104:107], v[48:51], v[200:203]\n\t"
    "v_mfma_f32_16x16x32_bf16 v[204:207], a[108:111], v[48:51], v[204:207]\n\t"
    "s_waitcnt lgkmcnt(10)\n\t"
    "v_mfma_f32_16x16x32_bf16 v[208:211], a[96:99], v[52:55], v[208:211]\n\t"
    "v_mfma_f32_16x16x32_bf16 v[212:215], a[100:103], v[52:55], v[212:215]\n\t"
    "v_mfma_f32_16x16x32_bf16 v[216:219], a[104:107], v[52:55], v[216:219]\n\t"
    "v_mfma_f32_16x16x32_bf16 v[220:223], a[108:111], v[52:55], v[220:223]\n\t"
    "s_waitcnt lgkmcnt(9)\n\t"
    "v_mfma_f32_16x16x32_bf16 v[224:227], a[96:99], v[56:59], v[224:227]\n\t"
    "v_mfma_f32_16x16x32_bf16 v[228:231], a[100:103], v[56:59], v[228:231]\n\t"
    "v_mfma_f32_16x16x32_bf16 v[232:235], a[104:107], v[56:59], v[232:235]\n\t"
    "v_mfma_f32_16x16x32_bf16 v[236:239], a[108:111], v[56:59], v[236:239]\n\t"
    "s_waitcnt lgkmcnt(8)\n\t"
    "v_mfma_f32_16x16x32_bf16 v[240:243], a[96:99], v[60:63], v[240:243]\n\t"
    "v_mfma_f32_16x16x32_bf16 v[244:247], a[100:103], v[60:63], v[244:247]\n\t"
    "v_mfma_f32_16x16x32_bf16 v[248:251], a[104:107], v[60:63], v[248:251]\n\t"
    "v_mfma_f32_16x16x32_bf16 v[252:255], a[108:111], v[60:63], v[252:255]\n\t"
    "s_waitcnt vmcnt(24)\n\t"
    "s_waitcnt lgkmcnt(0)\n\t"
    "s_barrier\n\t"
    "v_mfma_f32_16x16x32_bf16 v[128:131], a[112:115], v[64:67], v[128:131]\n\t"
    "ds_read_b128 v[32:35], v5 offset:0\n\t"
    "v_mfma_f32_16x16x32_bf16 v[132:135], a[116:119], v[64:67], v[132:135]\n\t"
    "v_mfma_f32_16x16x32_bf16 v[136:139], a[120:123], v[64:67], v[136:139]\n\t"
    "ds_read_b128 v[36:39], v5 offset:2048\n\t"
    "v_mfma_f32_16x16x32_bf16 v[140:143], a[124:127], v[64:67], v[140:143]\n\t"
    "s_add_u32 s46, s41, 384\n\t"
    "s_add_u32 m0, %[mb], 49152\n\t"
    "s_nop 0\n\t"
    "buffer_load_dwordx4 v0, %[rx], s46 offen lds\n\t"
    "v_mfma_f32_16x16x32_bf16 v[144:147], a[112:115], v[68:71], v[144:147]\n\t"
    "ds_read_b128 v[40:43], v5 offset:4096\n\t"
    "v_mfma_f32_16x16x32_bf16 v[148:151], a[116:119], v[68:71], v[148:151]\n\t"
    "v_mfma_f32_16x16x32_bf16 v[152:155], a[120:123], v[68:71], v[152:155]\n\t"
    "ds_read_b128 v[44:47], v5 offset:6144\n\t"
    "v_mfma_f32_16x16x32_bf16 v[156:159], a[124:127], v[68:71], v[156:159]\n\t"
    "v_mfma_f32_16x16x32_bf16 v[160:163], a[112:115], v[72:75], v[160:163]\n\t"
    "ds_read_b128 v[48:51], v5 offset:8192\n\t"
    "v_mfma_f32_16x16x32_bf16 v[164:167], a[116:119], v[72:75], v[164:167]\n\t"
    "s_add_u32 m0, %[mb], 50176\n\t"
    "s_nop 0\n\t"
    "buffer_load_dwordx4 v1, %[rx], s46 offen lds\n\t"
    "v_mfma_f32_16x16x32_bf16 v[168:171], a[120:123], v[72:75], v[168:171]\n\t"
    "ds_read_b128 v[52:55], v5 offset:10240\n\t"
    "v_mfma_f32_16x16x32_bf16 v[172:175], a[124:127], v[72:75], v[172:175]\n\t"
    "v_mfma_f32_16x16x32_bf16 v[176:179], a[112:115], v[76:79], v[176:179]\n\t"
    "ds_read_b128 v[56:59], v5 offset:12288\n\t"
    "v_mfma_f32_16x16x32_bf16 v[180:183], a[116:119], v[76:79], v[180:183]\n\t"
    "v_mfma_f32_16x16x32_bf16 v[184:187], a[120:123], v[76:79], v[184:187]\n\t"
    "ds_read_b128 v[60:63], v5 offset:14336\n\t"
    "v_mfma_f32_16x16x32_bf16 v[188:191], a[124:127], v[76:79], v[188:191]\n\t"
    "s_add_u32 m0, %[mb], 51200\n\t"
    "s_nop 0\n\t"
    "buffer_load_dwordx4 v2, %[rx], s46 offen lds\n\t"
    "v_mfma_f32_16x16x32_bf16 v[192:195], a[112:115], v[80:83], v[192:195]\n\t"
    "v_mfma_f32_16x16x32_bf16 v[196:199], a[116:119], v[80:83], v[196:199]\n\t"
    "v_mfma_f32_16x16x32_bf16 v[200:203], a[120:123], v[80:83], v[200:203]\n\t"
    "v_mfma_f32_16x16x32_bf16 v[204:207], a[124:127], v[80:83], v[204:207]\n\t"
    "v_mfma_f32_16x16x32_bf16 v[208:211], a[112:115], v[84:87], v[208:211]\n\t"
    "v_mfma_f32_16x16x32_bf16 v[212:215], a[116:119], v[84:87], v[212:215]\n\t"
    "s_add_u32 m0, %[mb], 52224\n\t"
    "s_nop 0\n\t"
    "buffer_load_dwordx4 v3, %[rx], s46 offen lds\n\t"
    "v_mfma_f32_16x16x32_bf16 v[216:219], a[120:123], v[84:87], v[216:219]\n\t"
    "v_mfma_f32_16x16x32_bf16 v[220:223], a[124:127], v[84:87], v[220:223]\n\t"
    "v_mfma_f32_16x16x32_bf16 v[224:227], a[112:115], v[88:91], v[224:227]\n\t"
    "v_mfma_f32_16x16x32_bf16 v[228:231], a[116:119], v[88:91], v[228:231]\n\t"
    "v_mfma_f32_16x16x32_bf16 v[232:235], a[120:123], v[88:91], v[232:235]\n\t"
    "v_mfma_f32_16x16x32_bf16 v[236:239], a[124:127], v[88:91], v[236:239]\n\t"
    "v_mfma_f32_16x16x32_bf16 v[240:243], a[112:115], v[92:95], v[240:243]\n\t"
    "v_mfma_f32_16x16x32_bf16 v[244:247], a[116:119], v[92:95], v[244:247]\n\t"
    "v_mfma_f32_16x16x32_bf16 v[248:251], a[120:123], v[92:95], v[248:251]\n\t"
    "v_mfma_f32_16x16x32_bf16 v[252:255], a[124:127], v[92:95], v[252:255]\n\t"
    "s_waitcnt lgkmcnt(7)\n\t"
    "v_mfma_f32_16x16x32_bf16 v[128:131], a[128:131], v[32:35], v[128:131]\n\t"
    "ds_read_b128 v[64:67], v7 offset:0\n\t"
    "v_mfma_f32_16x16x32_bf16 v[132:135], a[132:135], v[32:35], v[132:135]\n\t"
    "v_mfma_f32_16x16x32_bf16 v[136:139], a[136:139], v[32:35], v[136:139]\n\t"
    "ds_read_b128 v[68:71], v7 offset:2048\n\t"
    "v_mfma_f32_16x16x32_bf16 v[140:143], a[140:143], v[32:35], v[140:143]\n\t"
    "s_waitcnt lgkmcnt(8)\n\t"
    "v_mfma_f32_16x16x32_bf16 v[144:147], a[128:131], v[36:39], v[144:147]\n\t"
    "ds_read_b128 v[72:75], v7 offset:4096\n\t"
    "v_mfma_f32_16x16x32_bf16 v[148:151], a[132:135], v[36:39], v[148:151]\n\t"
    "v_mfma_f32_16x16x32_bf16 v[152:155], a[136:139], v[36:39], v[152:155]\n\t"
    "ds_read_b128 v[76:79], v7 offset:6144\n\t"
    "v_mfma_f32_16x16x32_bf16 v[156:159], a[140:143], v[36:39], v[156:159]\n\t"
    "s_waitcnt lgkmcnt(9)\n\t"
    "v_mfma_f32_16x16x32_bf16 v[160:163], a[128:131], v[40:43], v[160:163]\n\t"
    "ds_read_b128 v[80:83], v7 offset:8192\n\t"
    "v_mfma_f32_16x16x32_bf16 v[164:167], a[132:135], v[40:43], v[164:167]\n\t"
    "v_mfma_f32_16x16x32_bf16 v[168:171], a[136:139], v[40:43], v[168:171]\n\t"
    "ds_read_b128 v[84:87], v7 offset:10240\n\t"
    "v_mfma_f32_16x16x32_bf16 v[172:175], a[140:143], v[40:43], v[172:175]\n\t"
    "s_waitcnt lgkmcnt(10)\n\t"
    "v_mfma_f32_16x16x32_bf16 v[176:179], a[128:131], v[44:47], v[176:179]\n\t"
    "ds_read_b128 v[88:91], v7 offset:12288\n\t"
    "v_mfma_f32_16x16x32_bf16 v[180:183], a[132:135], v[44:47], v[180:183]\n\t"
    "v_mfma_f32_16x16x32_bf16 v[184:187], a[136:139], v[44:47], v[184:187]\n\t"
    "ds_read_b128 v[92:95], v7 offset:14336\n\t"
    "v_mfma_f32_16x16x32_bf16 v[188:191], a[140:143], v[44:47], v[188:191]\n\t"
    "s_waitcnt lgkmcnt(11)\n\t"
    "v_mfma_f32_16x16x32_bf16 v[192:195], a[128:131], v[48:51], v[192:195]\n\t"
    "v_mfma_f32_16x16x32_bf16 v[196:199], a[132:135], v[48:51], v[196:199]\n\t"
    "v_mfma_f32_16x16x32_bf16 v[200:203], a[136:139], v[48:51], v[200:203]\n\t"
    "v_mfma_f32_16x16x32_bf16 v[204:207], a[140:143], v[48:51], v[204:207]\n\t"
    "s_waitcnt lgkmcnt(10)\n\t"
    "v_mfma_f32_16x16x32_bf16 v[208:211], a[128:131], v[52:55], v[208:211]\n\t"
    "v_mfma_f32_16x16x32_bf16 v[212:215], a[132:135], v[52:55], v[212:215]\n\t"
    "v_mfma_f32_16x16x32_bf16 v[216:219], a[136:139], v[52:55], v[216:219]\n\t"
    "v_mfma_f32_16x16x32_bf16 v[220:223], a[140:143], v[52:55], v[220:223]\n\t"
    "s_waitcnt lgkmcnt(9)\n\t"
    "v_mfma_f32_16x16x32_bf16 v[224:227], a[128:131], v[56:59], v[224:227]\n\t"
    "v_mfma_f32_16x16x32_bf16 v[228:231], a[132:135], v[56:59], v[228:231]\n\t"
    "v_mfma_f32_16x16x32_bf16 v[232:235], a[136:139], v[56:59], v[232:235]\n\t"
    "v_mfma_f32_16x16x32_bf16 v[236:239], a[140:143], v[56:59], v[236:239]\n\t"
    "s_waitcnt lgkmcnt(8)\n\t"
    "v_mfma_f32_16x16x32_bf16 v[240:243], a[128:131], v[60:63], v[240:243]\n\t"
    "v_mfma_f32_16x16x32_bf16 v[244:247], a[132:135], v[60:63], v[244:247]\n\t"
    "v_mfma_f32_16x16x32_bf16 v[248:251], a[136:139], v[60:63], v[248:251]\n\t"
    "v_mfma_f32_16x16x32_bf16 v[252:255], a[140:143], v[60:63], v[252:255]\n\t"
    "s_waitcnt vmcnt(24)\n\t"
    "s_waitcnt lgkmcnt(0)\n\t"
    "s_barrier\n\t"
    "v_mfma_f32_16x16x32_bf16 v[128:131], a[144:147], v[64:67], v[128:131]\n\t"
    "ds_read_b128 v[32:35], v5 offset:16384\n\t"
    "v_mfma_f32_16x16x32_bf16 v[132:135], a[148:151], v[64:67], v[132:135]\n\t"
    "v_mfma_f32_16x16x32_bf16 v[136:139], a[152:155], v[64:67], v[136:139]\n\t"
    "ds_read_b128 v[36:39], v5 offset:18432\n\t"
    "v_mfma_f32_16x16x32_bf16 v[140:143], a[156:159], v[64:67], v[140:143]\n\t"
    "s_add_u32 s46, s41, 512\n\t"
    "s_add_u32 m0, %[mb], 65536\n\t"
    "s_nop 0\n\t"
    "buffer_load_dwordx4 v0, %[rx], s46 offen lds\n\t"
    "v_mfma_f32_16x16x32_bf16 v[144:147], a[144:147], v[68:71], v[144:147]\n\t"
    "ds_read_b128 v[40:43], v5 offset:20480\n\t"
    "v_mfma_f32_16x16x32_bf16 v[148:151], a[148:151], v[68:71], v[148:151]\n\t"
    "v_mfma_f32_16x16x32_bf16 v[152:155], a[152:155], v[68:71], v[152:155]\n\t"
    "ds_read_b128 v[44:47], v5 offset:22528\n\t"
    "v_mfma_f32_16x16x32_bf16 v[156:159], a[156:159], v[68:71], v[156:159]\n\t"
    "v_mfma_f32_16x16x32_bf16 v[160:163], a[144:147], v[72:75], v[160:163]\n\t"
    "ds_read_b128 v[48:51], v5 offset:24576\n\t"
    "v_mfma_f32_16x16x32_bf16 v[164:167], a[148:151], v[72:75], v[164:167]\n\t"
    "s_add_u32 m0, %[mb], 66560\n\t"
    "s_nop 0\n\t"
    "buffer_load_dwordx4 v1, %[rx], s46 offen lds\n\t"
    "v_mfma_f32_16x16x32_bf16 v[168:171], a[152:155], v[72:75], v[168:171]\n\t"
    "ds_read_b128 v[52:55], v5 offset:26624\n\t"
    "v_mfma_f32_16x16x32_bf16 v[172:175], a[156:159], v[72:75], v[172:175]\n\t"
    "v_mfma_f32_16x16x32_bf16 v[176:179], a[144:147], v[76:79], v[176:179]\n\t"
    "ds_read_b128 v[56:59], v5 offset:28672\n\t"
    "v_mfma_f32_16x16x32_bf16 v[180:183], a[148:151], v[76:79], v[180:183]\n\t"
    "v_mfma_f32_16x16x32_bf16 v[184:187], a[152:155], v[76:79], v[184:187]\n\t"
    "ds_read_b128 v[60:63], v5 offset:30720\n\t"
    "v_mfma_f32_16x16x32_bf16 v[188:191], a[156:159], v[76:79], v[188:191]\n\t"
    "s_add_u32 m0, %[mb], 67584\n\t"
    "s_nop 0\n\t"
    "buffer_load_dwordx4 v2, %[rx], s46 offen lds\n\t"
    "v_mfma_f32_16x16x32_bf16 v[192:195], a[144:147], v[80:83], v[192:195]\n\t"
    "v_mfma_f32_16x16x32_bf16 v[196:199], a[148:151], v[80:83], v[196:199]\n\t"
    "v_mfma_f32_16x16x32_bf16 v[200:203], a[152:155], v[80:83], v[200:203]\n\t"
    "v_mfma_f32_16x16x32_bf16 v[204:207], a[156:159], v[80:83], v[204:207]\n\t"
    "v_mfma_f32_16x16x32_bf16 v[208:211], a[144:147], v[84:87], v[208:211]\n\t"
    "v_mfma_f32_16x16x32_bf16 v[212:215], a[148:151], v[84:87], v[212:215]\n\t"
    "s_add_u32 m0, %[mb], 68608\n\t"
    "s_nop 0\n\t"
    "buffer_load_dwordx4 v3, %[rx], s46 offen lds\n\t"
    "v_mfma_f32_16x16x32_bf16 v[216:219], a[152:155], v[84:87], v[216:219]\n\t"
    "v_mfma_f32_16x16x32_bf16 v[220:223], a[156:159], v[84:87], v[220:223]\n\t"
    "v_mfma_f32_16x16x32_bf16 v[224:227], a[144:147], v[88:91], v[224:227]\n\t"
    "v_mfma_f32_16x16x32_bf16 v[228:231], a[148:151], v[88:91], v[228:231]\n\t"
    "v_mfma_f32_16x16x32_bf16 v[232:235], a[152:155], v[88:91], v[232:235]\n\t"
    "v_mfma_f32_16x16x32_bf16 v[236:239], a[156:159], v[88:91], v[236:239]\n\t"
    "v_mfma_f32_16x16x32_bf16 v[240:243], a[144:147], v[92:95], v[240:243]\n\t"
    "v_mfma_f32_16x16x32_bf16 v[244:247], a[148:151], v[92:95], v[244:247]\n\t"
    "v_mfma_f32_16x16x32_bf16 v[248:251], a[152:155], v[92:95], v[248:251]\n\t"
    "v_mfma_f32_16x16x32_bf16 v[252:255], a[156:159], v[92:95], v[252:255]\n\t"
    "s_waitcnt lgkmcnt(7)\n\t"
    "v_mfma_f32_16x16x32_bf16 v[128:131], a[160:163], v[32:35], v[128:131]\n\t"
    "ds_read_b128 v[64:67], v7 offset:16384\n\t"
    "v_mfma_f32_16x16x32_bf16 v[132:135], a[164:167], v[32:35], v[132:135]\n\t"
    "v_mfma_f32_16x16x32_bf16 v[136:139], a[168:171], v[32:35], v[136:139]\n\t"
    "ds_read_b128 v[68:71], v7 offset:18432\n\t"
    "v_mfma_f32_16x16x32_bf16 v[140:143], a[172:175], v[32:35], v[140:143]\n\t"
    "s_waitcnt lgkmcnt(8)\n\t"
    "v_mfma_f32_16x16x32_bf16 v[144:147], a[160:163], v[36:39], v[144:147]\n\t"
    "ds_read_b128 v[72:75], v7 offset:20480\n\t"
    "v_mfma_f32_16x16x32_bf16 v[148:151], a[164:167], v[36:39], v[148:151]\n\t"
    "v_mfma_f32_16x16x32_bf16 v[152:155], a[168:171], v[36:39], v[152:155]\n\t"
    "ds_read_b128 v[76:79], v7 offset:22528\n\t"
    "v_mfma_f32_16x16x32_bf16 v[156:159], a[172:175], v[36:39], v[156:159]\n\t"
    "s_waitcnt lgkmcnt(9)\n\t"
    "v_mfma_f32_16x16x32_bf16 v[160:163], a[160:163], v[40:43], v[160:163]\n\t"
    "ds_read_b128 v[80:83], v7 offset:24576\n\t"
    "v_mfma_f32_16x16x32_bf16 v[164:167], a[164:167], v[40:43], v[164:167]\n\t"
    "v_mfma_f32_16x16x32_bf16 v[168:171], a[168:171], v[40:43], v[168:171]\n\t"
    "ds_read_b128 v[84:87], v7 offset:26624\n\t"
    "v_mfma_f32_16x16x32_bf16 v[172:175], a[172:175], v[40:43], v[172:175]\n\t"
    "s_waitcnt lgkmcnt(10)\n\t"
    "v_mfma_f32_16x16x32_bf16 v[176:179], a[160:163], v[44:47], v[176:179]\n\t"
    "ds_read_b128 v[88:91], v7 offset:28672\n\t"
    "v_mfma_f32_16x16x32_bf16 v[180:183], a[164:167], v[44:47], v[180:183]\n\t"
    "v_mfma_f32_16x16x32_bf16 v[184:187], a[168:171], v[44:47], v[184:187]\n\t"
    "ds_read_b128 v[92:95], v7 offset:30720\n\t"
    "v_mfma_f32_16x16x32_bf16 v[188:191], a[172:175], v[44:47], v[188:191]\n\t"
    "s_waitcnt lgkmcnt(11)\n\t"
    "v_mfma_f32_16x16x32_bf16 v[192:195], a[160:163], v[48:51], v[192:195]\n\t"
    "v_mfma_f32_16x16x32_bf16 v[196:199], a[164:167], v[48:51], v[196:199]\n\t"
    "v_mfma_f32_16x16x32_bf16 v[200:203], a[168:171], v[48:51], v[200:203]\n\t"
    "v_mfma_f32_16x16x32_bf16 v[204:207], a[172:175], v[48:51], v[204:207]\n\t"
    "s_waitcnt lgkmcnt(10)\n\t"
    "v_mfma_f32_16x16x32_bf16 v[208:211], a[160:163], v[52:55], v[208:211]\n\t"
    "v_mfma_f32_16x16x32_bf16 v[212:215], a[164:167], v[52:55], v[212:215]\n\t"
    "v_mfma_f32_16x16x32_bf16 v[216:219], a[168:171], v[52:55], v[216:219]\n\t"
    "v_mfma_f32_16x16x32_bf16 v[220:223], a[172:175], v[52:55], v[220:223]\n\t"
    "s_waitcnt lgkmcnt(9)\n\t"
    "v_mfma_f32_16x16x32_bf16 v[224:227], a[160:163], v[56:59], v[224:227]\n\t"
    "v_mfma_f32_16x16x32_bf16 v[228:231], a[164:167], v[56:59], v[228:231]\n\t"
    "v_mfma_f32_16x16x32_bf16 v[232:235], a[168:171], v[56:59], v[232:235]\n\t"
    "v_mfma_f32_16x16x32_bf16 v[236:239], a[172:175], v[56:59], v[236:239]\n\t"
    "s_waitcnt lgkmcnt(8)\n\t"
    "v_mfma_f32_16x16x32_bf16 v[240:243], a[160:163], v[60:63], v[240:243]\n\t"
    "v_mfma_f32_16x16x32_bf16 v[244:247], a[164:167], v[60:63], v[244:247]\n\t"
    "v_mfma_f32_16x16x32_bf16 v[248:251], a[168:171], v[60:63], v[248:251]\n\t"
    "v_mfma_f32_16x16x32_bf16 v[252:255], a[172:175], v[60:63], v[252:255]\n\t"
    "s_waitcnt vmcnt(24)\n\t"
    "s_waitcnt lgkmcnt(0)\n\t"
    "s_barrier\n\t"
    "v_mfma_f32_16x16x32_bf16 v[128:131], a[176:179], v[64:67], v[128:131]\n\t"
    "ds_read_b128 v[32:35], v5 offset:32768\n\t"
    "v_mfma_f32_16x16x32_bf16 v[132:135], a[180:183], v[64:67], v[132:135]\n\t"
    "v_mfma_f32_16x16x32_bf16 v[136:139], a[184:187], v[64:67], v[136:139]\n\t"
    "ds_read_b128 v[36:39], v5 offset:34816\n\t"
    "v_mfma_f32_16x16x32_bf16 v[140:143], a[188:191], v[64:67], v[140:143]\n\t"
    "s_add_u32 s46, s41, 640\n\t"
    "s_add_u32 m0, %[mb], 81920\n\t"
    "s_nop 0\n\t"
    "buffer_load_dwordx4 v0, %[rx], s46 offen lds\n\t"
    "v_mfma_f32_16x16x32_bf16 v[144:147], a[176:179], v[68:71], v[144:147]\n\t"
    "ds_read_b128 v[40:43], v5 offset:36864\n\t"
    "v_mfma_f32_16x16x32_bf16 v[148:151], a[180:183], v[68:71], v[148:151]\n\t"
    "v_mfma_f32_16x16x32_bf16 v[152:155], a[184:187], v[68:71], v[152:155]\n\t"
    "ds_read_b128 v[44:47], v5 offset:38912\n\t"
    "v_mfma_f32_16x16x32_bf16 v[156:159], a[188:191], v[68:71], v[156:159]\n\t"
    "v_mfma_f32_16x16x32_bf16 v[160:163], a[176:179], v[72:75], v[160:163]\n\t"
    "ds_read_b128 v[48:51], v5 offset:40960\n\t"
    "v_mfma_f32_16x16x32_bf16 v[164:167], a[180:183], v[72:75], v[164:167]\n\t"
    "s_add_u32 m0, %[mb], 82944\n\t"
    "s_nop 0\n\t"
    "buffer_load_dwordx4 v1, %[rx], s46 offen lds\n\t"
    "v_mfma_f32_16x16x32_bf16 v[168:171], a[184:187], v[72:75], v[168:171]\n\t"
    "ds_read_b128 v[52:55], v5 offset:43008\n\t"
    "v_mfma_f32_16x16x32_bf16 v[172:175], a[188:191], v[72:75], v[172:175]\n\t"
    "v_mfma_f32_16x16x32_bf16 v[176:179], a[176:179], v[76:79], v[176:179]\n\t"
    "ds_read_b128 v[56:59], v5 offset:45056\n\t"
    "v_mfma_f32_16x16x32_bf16 v[180:183], a[180:183], v[76:79], v[180:183]\n\t"
    "v_mfma_f32_16x16x32_bf16 v[184:187], a[184:187], v[76:79], v[184:187]\n\t"
    "ds_read_b128 v[60:63], v5 offset:47104\n\t"
    "v_mfma_f32_16x16x32_bf16 v[188:191], a[188:191], v[76:79], v[188:191]\n\t"
    "s_add_u32 m0, %[mb], 83968\n\t"
    "s_nop 0\n\t"
    "buffer_load_dwordx4 v2, %[rx], s46 offen lds\n\t"
    "v_mfma_f32_16x16x32_bf16 v[192:195], a[176:179], v[80:83], v[192:195]\n\t"
    "v_mfma_f32_16x16x32_bf16 v[196:199], a[180:183], v[80:83], v[196:199]\n\t"
    "v_mfma_f32_16x16x32_bf16 v[200:203], a[184:187], v[80:83], v[200:203]\n\t"
    "v_mfma_f32_16x16x32_bf16 v[204:207], a[188:191], v[80:83], v[204:207]\n\t"
    "v_mfma_f32_16x16x32_bf16 v[208:211], a[176:179], v[84:87], v[208:211]\n\t"
    "v_mfma_f32_16x16x32_bf16 v[212:215], a[180:183], v[84:87], v[212:215]\n\t"
    "s_add_u32 m0, %[mb], 84992\n\t"
    "s_nop 0\n\t"
    "buffer_load_dwordx4 v3, %[rx], s46 offen lds\n\t"
    "v_mfma_f32_16x16x32_bf16 v[216:219], a[184:187], v[84:87], v[216:219]\n\t"
    "v_mfma_f32_16x16x32_bf16 v[220:223], a[188:191], v[84:87], v[220:223]\n\t"
    "v_mfma_f32_16x16x32_bf16 v[224:227], a[176:179], v[88:91], v[224:227]\n\t"
    "v_mfma_f32_16x16x32_bf16 v[228:231], a[180:183], v[88:91], v[228:231]\n\t"
    "v_mfma_f32_16x16x32_bf16 v[232:235], a[184:187], v[88:91], v[232:235]\n\t"
    "v_mfma_f32_16x16x32_bf16 v[236:239], a[188:191], v[88:91], v[236:239]\n\t"
    "v_mfma_f32_16x16x32_bf16 v[240:243], a[176:179], v[92:95], v[240:243]\n\t"
    "v_mfma_f32_16x16x32_bf16 v[244:247], a[180:183], v[92:95], v[244:247]\n\t"
    "v_mfma_f32_16x16x32_bf16 v[248:251], a[184:187], v[92:95], v[248:251]\n\t"
    "v_mfma_f32_16x16x32_bf16 v[252:255], a[188:191], v[92:95], v[252:255]\n\t"
    "s_waitcnt lgkmcnt(7)\n\t"
    "v_mfma_f32_16x16x32_bf16 v[128:131], a[192:195], v[32:35], v[128:131]\n\t"
    "ds_read_b128 v[64:67], v7 offset:32768\n\t"
    "v_mfma_f32_16x16x32_bf16 v[132:135], a[196:199], v[32:35], v[132:135]\n\t"
    "v_mfma_f32_16x16x32_bf16 v[136:139], a[200:203], v[32:35], v[136:139]\n\t"
    "ds_read_b128 v[68:71], v7 offset:34816\n\t"
    "v_mfma_f32_16x16x32_bf16 v[140:143], a[204:207], v[32:35], v[140:143]\n\t"
    "s_waitcnt lgkmcnt(8)\n\t"
    "v_mfma_f32_16x16x32_bf16 v[144:147], a[192:195], v[36:39], v[144:147]\n\t"
    "ds_read_b128 v[72:75], v7 offset:36864\n\t"
    "v_mfma_f32_16x16x32_bf16 v[148:151], a[196:199], v[36:39], v[148:151]\n\t"
    "v_mfma_f32_16x16x32_bf16 v[152:155], a[200:203], v[36:39], v[152:155]\n\t"
    "ds_read_b128 v[76:79], v7 offset:38912\n\t"
    "v_mfma_f32_16x16x32_bf16 v[156:159], a[204:207], v[36:39], v[156:159]\n\t"
    "s_waitcnt lgkmcnt(9)\n\t"
    "v_mfma_f32_16x16x32_bf16 v[160:163], a[192:195], v[40:43], v[160:163]\n\t"
    "ds_read_b128 v[80:83], v7 offset:40960\n\t"
    "v_mfma_f32_16x16x32_bf16 v[164:167], a[196:199], v[40:43], v[164:167]\n\t"
    "v_mfma_f32_16x16x32_bf16 v[168:171], a[200:203], v[40:43], v[168:171]\n\t"
    "ds_read_b128 v[84:87], v7 offset:43008\n\t"
    "v_mfma_f32_16x16x32_bf16 v[172:175], a[204:207], v[40:43], v[172:175]\n\t"
    "s_waitcnt lgkmcnt(10)\n\t"
    "v_mfma_f32_16x16x32_bf16 v[176:179], a[192:195], v[44:47], v[176:179]\n\t"
    "ds_read_b128 v[88:91], v7 offset:45056\n\t"
    "v_mfma_f32_16x16x32_bf16 v[180:183], a[196:199], v[44:47], v[180:183]\n\t"
    "v_mfma_f32_16x16x32_bf16 v[184:187], a[200:203], v[44:47], v[184:187]\n\t"
    "ds_read_b128 v[92:95], v7 offset:47104\n\t"
    "v_mfma_f32_16x16x32_bf16 v[188:191], a[204:207], v[44:47], v[188:191]\n\t"
    "s_waitcnt lgkmcnt(11)\n\t"
    "v_mfma_f32_16x16x32_bf16 v[192:195], a[192:195], v[48:51], v[192:195]\n\t"
    "v_mfma_f32_16x16x32_bf16 v[196:199], a[196:199], v[48:51], v[196:199]\n\t"
    "v_mfma_f32_16x16x32_bf16 v[200:203], a[200:203], v[48:51], v[200:203]\n\t"
    "v_mfma_f32_16x16x32_bf16 v[204:207], a[204:207], v[48:51], v[204:207]\n\t"
    "s_waitcnt lgkmcnt(10)\n\t"
    "v_mfma_f32_16x16x32_bf16 v[208:211], a[192:195], v[52:55], v[208:211]\n\t"
    "v_mfma_f32_16x16x32_bf16 v[212:215], a[196:199], v[52:55], v[212:215]\n\t"
    "v_mfma_f32_16x16x32_bf16 v[216:219], a[200:203], v[52:55], v[216:219]\n\t"
    "v_mfma_f32_16x16x32_bf16 v[220:223], a[204:207], v[52:55], v[220:223]\n\t"
    "s_waitcnt lgkmcnt(9)\n\t"
    "v_mfma_f32_16x16x32_bf16 v[224:227], a[192:195], v[56:59], v[224:227]\n\t"
    "v_mfma_f32_16x16x32_bf16 v[228:231], a[196:199], v[56:59], v[228:231]\n\t"
    "v_mfma_f32_16x16x32_bf16 v[232:235], a[200:203], v[56:59], v[232:235]\n\t"
    "v_mfma_f32_16x16x32_bf16 v[236:239], a[204:207], v[56:59], v[236:239]\n\t"
    "s_waitcnt lgkmcnt(8)\n\t"
    "v_mfma_f32_16x16x32_bf16 v[240:243], a[192:195], v[60:63], v[240:243]\n\t"
    "v_mfma_f32_16x16x32_bf16 v[244:247], a[196:199], v[60:63], v[244:247]\n\t"
    "v_mfma_f32_16x16x32_bf16 v[248:251], a[200:203], v[60:63], v[248:251]\n\t"
    "v_mfma_f32_16x16x32_bf16 v[252:255], a[204:207], v[60:63], v[252:255]\n\t"
    "s_waitcnt vmcnt(24)\n\t"
    "s_waitcnt lgkmcnt(0)\n\t"
    "s_barrier\n\t"
    "v_mfma_f32_16x16x32_bf16 v[128:131], a[208:211], v[64:67], v[128:131]\n\t"
    "ds_read_b128 v[32:35], v5 offset:49152\n\t"
    "v_mfma_f32_16x16x32_bf16 v[132:135], a[212:215], v[64:67], v[132:135]\n\t"
    "v_mfma_f32_16x16x32_bf16 v[136:139], a[216:219], v[64:67], v[136:139]\n\t"
    "ds_read_b128 v[36:39], v5 offset:51200\n\t"
    "v_mfma_f32_16x16x32_bf16 v[140:143], a[220:223], v[64:67], v[140:143]\n\t"
    "s_add_u32 s46, s41, 768\n\t"
    "s_add_u32 m0, %[mb], 98304\n\t"
    "s_nop 0\n\t"
    "buffer_load_dwordx4 v0, %[rx], s46 offen lds\n\t"
    "v_mfma_f32_16x16x32_bf16 v[144:147], a[208:211], v[68:71], v[144:147]\n\t"
    "ds_read_b128 v[40:43], v5 offset:53248\n\t"
    "v_mfma_f32_16x16x32_bf16 v[148:151], a[212:215], v[68:71], v[148:151]\n\t"
    "v_mfma_f32_16x16x32_bf16 v[152:155], a[216:219], v[68:71], v[152:155]\n\t"
    "ds_read_b128 v[44:47], v5 offset:55296\n\t"
    "v_mfma_f32_16x16x32_bf16 v[156:159], a[220:223], v[68:71], v[156:159]\n\t"
    "v_mfma_f32_16x16x32_bf16 v[160:163], a[208:211], v[72:75], v[160:163]\n\t"
    "ds_read_b128 v[48:51], v5 offset:57344\n\t"
    "v_mfma_f32_16x16x32_bf16 v[164:167], a[212:215], v[72:75], v[164:167]\n\t"
    "s_add_u32 m0, %[mb], 99328\n\t"
    "s_nop 0\n\t"
    "buffer_load_dwordx4 v1, %[rx], s46 offen lds\n\t"
    "v_mfma_f32_16x16x32_bf16 v[168:171], a[216:219], v[72:75], v[168:171]\n\t"
    "ds_read_b128 v[52:55], v5 offset:59392\n\t"
    "v_mfma_f32_16x16x32_bf16 v[172:175], a[220:223], v[72:75], v[172:175]\n\t"
    "v_mfma_f32_16x16x32_bf16 v[176:179], a[208:211], v[76:79], v[176:179]\n\t"
    "ds_read_b128 v[56:59], v5 offset:61440\n\t"
    "v_mfma_f32_16x16x32_bf16 v[180:183], a[212:215], v[76:79], v[180:183]\n\t"
    "v_mfma_f32_16x16x32_bf16 v[184:187], a[216:219], v[76:79], v[184:187]\n\t"
    "ds_read_b128 v[60:63], v5 offset:63488\n\t"
    "v_mfma_f32_16x16x32_bf16 v[188:191], a[220:223], v[76:79], v[188:191]\n\t"
    "s_add_u32 m0, %[mb], 100352\n\t"
    "s_nop 0\n\t"
    "buffer_load_dwordx4 v2, %[rx], s46 offen lds\n\t"
    "v_mfma_f32_16x16x32_bf16 v[192:195], a[208:211], v[80:83], v[192:195]\n\t"
    "v_mfma_f32_16x16x32_bf16 v[196:199], a[212:215], v[80:83], v[196:199]\n\t"
    "v_mfma_f32_16x16x32_bf16 v[200:203], a[216:219], v[80:83], v[200:203]\n\t"
    "v_mfma_f32_16x16x32_bf16 v[204:207], a[220:223], v[80:83], v[204:207]\n\t"
    "v_mfma_f32_16x16x32_bf16 v[208:211], a[208:211], v[84:87], v[208:211]\n\t"
    "v_mfma_f32_16x16x32_bf16 v[212:215], a[212:215], v[84:87], v[212:215]\n\t"
    "s_add_u32 m0, %[mb], 101376\n\t"
    "s_nop 0\n\t"
    "buffer_load_dwordx4 v3, %[rx], s46 offen lds\n\t"
    "v_mfma_f32_16x16x32_bf16 v[216:219], a[216:219], v[84:87], v[216:219]\n\t"
    "v_mfma_f32_16x16x32_bf16 v[220:223], a[220:223], v[84:87], v[220:223]\n\t"
    "v_mfma_f32_16x16x32_bf16 v[224:227], a[208:211], v[88:91], v[224:227]\n\t"
    "v_mfma_f32_16x16x32_bf16 v[228:231], a[212:215], v[88:91], v[228:231]\n\t"
    "v_mfma_f32_16x16x32_bf16 v[232:235], a[216:219], v[88:91], v[232:235]\n\t"
    "v_mfma_f32_16x16x32_bf16 v[236:239], a[220:223], v[88:91], v[236:239]\n\t"
    "v_mfma_f32_16x16x32_bf16 v[240:243], a[208:211], v[92:95], v[240:243]\n\t"
    "v_mfma_f32_16x16x32_bf16 v[244:247], a[212:215], v[92:95], v[244:247]\n\t"
    "v_mfma_f32_16x16x32_bf16 v[248:251], a[216:219], v[92:95], v[248:251]\n\t"
    "v_mfma_f32_16x16x32_bf16 v[252:255], a[220:223], v[92:95], v[252:255]\n\t"
    "s_waitcnt lgkmcnt(7)\n\t"
    "v_mfma_f32_16x16x32_bf16 v[128:131], a[224:227], v[32:35], v[128:131]\n\t"
    "ds_read_b128 v[64:67], v7 offset:49152\n\t"
    "v_mfma_f32_16x16x32_bf16 v[132:135], a[228:231], v[32:35], v[132:135]\n\t"
    "v_mfma_f32_16x16x32_bf16 v[136:139], a[232:235], v[32:35], v[136:139]\n\t"
    "ds_read_b128 v[68:71], v7 offset:51200\n\t"
    "v_mfma_f32_16x16x32_bf16 v[140:143], a[236:239], v[32:35], v[140:143]\n\t"
    "s_waitcnt lgkmcnt(8)\n\t"
    "v_mfma_f32_16x16x32_bf16 v[144:147], a[224:227], v[36:39], v[144:147]\n\t"
    "ds_read_b128 v[72:75], v7 offset:53248\n\t"
    "v_mfma_f32_16x16x32_bf16 v[148:151], a[228:231], v[36:39], v[148:151]\n\t"
    "v_mfma_f32_16x16x32_bf16 v[152:155], a[232:235], v[36:39], v[152:155]\n\t"
    "ds_read_b128 v[76:79], v7 offset:55296\n\t"
    "v_mfma_f32_16x16x32_bf16 v[156:159], a[236:239], v[36:39], v[156:159]\n\t"
    "s_waitcnt lgkmcnt(9)\n\t"
    "v_mfma_f32_16x16x32_bf16 v[160:163], a[224:227], v[40:43], v[160:163]\n\t"
    "ds_read_b128 v[80:83], v7 offset:57344\n\t"
    "v_mfma_f32_16x16x32_bf16 v[164:167], a[228:231], v[40:43], v[164:167]\n\t"
    "v_mfma_f32_16x16x32_bf16 v[168:171], a[232:235], v[40:43], v[168:171]\n\t"
    "ds_read_b128 v[84:87], v7 offset:59392\n\t"
    "v_mfma_f32_16x16x32_bf16 v[172:175], a[236:239], v[40:43], v[172:175]\n\t"
    "s_waitcnt lgkmcnt(10)\n\t"
    "v_mfma_f32_16x16x32_bf16 v[176:179], a[224:227], v[44:47], v[176:179]\n\t"
    "ds_read_b128 v[88:91], v7 offset:61440\n\t"
    "v_mfma_f32_16x16x32_bf16 v[180:183], a[228:231], v[44:47], v[180:183]\n\t"
    "v_mfma_f32_16x16x32_bf16 v[184:187], a[232:235], v[44:47], v[184:187]\n\t"
    "ds_read_b128 v[92:95], v7 offset:63488\n\t"
    "v_mfma_f32_16x16x32_bf16 v[188:191], a[236:239], v[44:47], v[188:191]\n\t"
    "s_waitcnt lgkmcnt(11)\n\t"
    "v_mfma_f32_16x16x32_bf16 v[192:195], a[224:227], v[48:51], v[192:195]\n\t"
    "v_mfma_f32_16x16x32_bf16 v[196:199], a[228:231], v[48:51], v[196:199]\n\t"
    "v_mfma_f32_16x16x32_bf16 v[200:203], a[232:235], v[48:51], v[200:203]\n\t"
    "v_mfma_f32_16x16x32_bf16 v[204:207], a[236:239], v[48:51], v[204:207]\n\t"
    "s_waitcnt lgkmcnt(10)\n\t"
    "v_mfma_f32_16x16x32_bf16 v[208:211], a[224:227], v[52:55], v[208:211]\n\t"
    "v_mfma_f32_16x16x32_bf16 v[212:215], a[228:231], v[52:55], v[212:215]\n\t"
    "v_mfma_f32_16x16x32_bf16 v[216:219], a[232:235], v[52:55], v[216:219]\n\t"
    "v_mfma_f32_16x16x32_bf16 v[220:223], a[236:239], v[52:55], v[220:223]\n\t"
    "s_waitcnt lgkmcnt(9)\n\t"
    "v_mfma_f32_16x16x32_bf16 v[224:227], a[224:227], v[56:59], v[224:227]\n\t"
    "v_mfma_f32_16x16x32_bf16 v[228:231], a[228:231], v[56:59], v[228:231]\n\t"
    "v_mfma_f32_16x16x32_bf16 v[232:235], a[232:235], v[56:59], v[232:235]\n\t"
    "v_mfma_f32_16x16x32_bf16 v[236:239], a[236:239], v[56:59], v[236:239]\n\t"
    "s_waitcnt lgkmcnt(8)\n\t"
    "v_mfma_f32_16x16x32_bf16 v[240:243], a[224:227], v[60:63], v[240:243]\n\t"
    "v_mfma_f32_16x16x32_bf16 v[244:247], a[228:231], v[60:63], v[244:247]\n\t"
    "v_mfma_f32_16x16x32_bf16 v[248:251], a[232:235], v[60:63], v[248:251]\n\t"
    "v_mfma_f32_16x16x32_bf16 v[252:255], a[236:239], v[60:63], v[252:255]\n\t"
    "s_waitcnt vmcnt(24)\n\t"
    "s_waitcnt lgkmcnt(0)\n\t"
    "s_barrier\n\t"
    "v_mfma_f32_16x16x32_bf16 v[128:131], a[240:243], v[64:67], v[128:131]\n\t"
    "ds_read_b128 v[32:35], v4 offset:0\n\t"
    "v_mfma_f32_16x16x32_bf16 v[132:135], a[244:247], v[64:67], v[132:135]\n\t"
    "v_mfma_f32_16x16x32_bf16 v[136:139], a[248:251], v[64:67], v[136:139]\n\t"
    "ds_read_b128 v[36:39], v4 offset:2048\n\t"
    "v_mfma_f32_16x16x32_bf16 v[140:143], a[252:255], v[64:67], v[140:143]\n\t"
    "s_add_u32 s46, s41, 896\n\t"
    "s_add_u32 m0, %[mb], 114688\n\t"
    "s_nop 0\n\t"
    "buffer_load_dwordx4 v0, %[rx], s46 offen lds\n\t"
    "v_mfma_f32_16x16x32_bf16 v[144:147], a[240:243], v[68:71], v[144:147]\n\t"
    "ds_read_b128 v[40:43], v4 offset:4096\n\t"
    "v_mfma_f32_16x16x32_bf16 v[148:151], a[244:247], v[68:71], v[148:151]\n\t"
    "v_mfma_f32_16x16x32_bf16 v[152:155], a[248:251], v[68:71], v[152:155]\n\t"
    "ds_read_b128 v[44:47], v4 offset:6144\n\t"
    "v_mfma_f32_16x16x32_bf16 v[156:159], a[252:255], v[68:71], v[156:159]\n\t"
    "v_mfma_f32_16x16x32_bf16 v[160:163], a[240:243], v[72:75], v[160:163]\n\t"
    "ds_read_b128 v[48:51], v4 offset:8192\n\t"
    "v_mfma_f32_16x16x32_bf16 v[164:167], a[244:247], v[72:75], v[164:167]\n\t"
    "s_add_u32 m0, %[mb], 115712\n\t"
    "s_nop 0\n\t"
    "buffer_load_dwordx4 v1, %[rx], s46 offen lds\n\t"
    "v_mfma_f32_16x16x32_bf16 v[168:171], a[248:251], v[72:75], v[168:171]\n\t"
    "ds_read_b128 v[52:55], v4 offset:10240\n\t"
    "v_mfma_f32_16x16x32_bf16 v[172:175], a[252:255], v[72:75], v[172:175]\n\t"
    "v_mfma_f32_16x16x32_bf16 v[176:179], a[240:243], v[76:79], v[176:179]\n\t"
    "ds_read_b128 v[56:59], v4 offset:12288\n\t"
    "v_mfma_f32_16x16x32_bf16 v[180:183], a[244:247], v[76:79], v[180:183]\n\t"
    "v_mfma_f32_16x16x32_bf16 v[184:187], a[248:251], v[76:79], v[184:187]\n\t"
    "ds_read_b128 v[60:63], v4 offset:14336\n\t"
    "v_mfma_f32_16x16x32_bf16 v[188:191], a[252:255], v[76:79], v[188:191]\n\t"
    "s_add_u32 m0, %[mb], 116736\n\t"
    "s_nop 0\n\t"
    "buffer_load_dwordx4 v2, %[rx], s46 offen lds\n\t"
    "v_mfma_f32_16x16x32_bf16 v[192:195], a[240:243], v[80:83], v[192:195]\n\t"
    "v_mfma_f32_16x16x32_bf16 v[196:199], a[244:247], v[80:83], v[196:199]\n\t"
    "v_mfma_f32_16x16x32_bf16 v[200:203], a[248:251], v[80:83], v[200:203]\n\t"
    "v_mfma_f32_16x16x32_bf16 v[204:207], a[252:255], v[80:83], v[204:207]\n\t"
    "v_mfma_f32_16x16x32_bf16 v[208:211], a[240:243], v[84:87], v[208:211]\n\t"
    "v_mfma_f32_16x16x32_bf16 v[212:215], a[244:247], v[84:87], v[212:215]\n\t"
    "s_add_u32 m0, %[mb], 117760\n\t"
    "s_nop 0\n\t"
    "buffer_load_dwordx4 v3, %[rx], s46 offen lds\n\t"
    "v_mfma_f32_16x16x32_bf16 v[216:219], a[248:251], v[84:87], v[216:219]\n\t"
    "v_mfma_f32_16x16x32_bf16 v[220:223], a[252:255], v[84:87], v[220:223]\n\t"
    "v_mfma_f32_16x16x32_bf16 v[224:227], a[240:243], v[88:91], v[224:227]\n\t"
    "v_mfma_f32_16x16x32_bf16 v[228:231], a[244:247], v[88:91], v[228:231]\n\t"
    "v_mfma_f32_16x16x32_bf16 v[232:235], a[248:251], v[88:91], v[232:235]\n\t"
    "v_mfma_f32_16x16x32_bf16 v[236:239], a[252:255], v[88:91], v[236:239]\n\t"
    "v_mfma_f32_16x16x32_bf16 v[240:243], a[240:243], v[92:95], v[240:243]\n\t"
    "v_mfma_f32_16x16x32_bf16 v[244:247], a[244:247], v[92:95], v[244:247]\n\t"
    "v_mfma_f32_16x16x32_bf16 v[248:251], a[248:251], v[92:95], v[248:251]\n\t"
    "v_mfma_f32_16x16x32_bf16 v[252:255], a[252:255], v[92:95], v[252:255]\n\t"
    "s_cmp_lt_u32 %[act], 48\n\t"
    "s_cbranch_scc1 3f\n\t"
    "s_nop 7\n\t"
    "v_cvt_pk_bf16_f32 v96, v128, v129\n\t"
    "v_lshlrev_b32 v128, 16, v96\n\t"
    "v_and_b32 v129, 0xffff0000, v96\n\t"
    "v_mul_f32 v97, 0xbfb8aa3b, v128\n\t"
    "v_mul_f32 v98, 0xbfb8aa3b, v129\n\t"
    "v_exp_f32 v97, v97\n\t"
    "v_exp_f32 v98, v98\n\t"
    "v_add_f32 v97, 1.0, v97\n\t"
    "v_add_f32 v98, 1.0, v98\n\t"
    "v_rcp_f32 v97, v97\n\t"
    "v_rcp_f32 v98, v98\n\t"
    "v_mul_f32 v128, v128, v97\n\t"
    "v_mul_f32 v129, v129, v98\n\t"
    "v_cvt_pk_bf16_f32 v96, v130, v131\n\t"
    "v_lshlrev_b32 v130, 16, v96\n\t"
    "v_and_b32 v131, 0xffff0000, v96\n\t"
    "v_mul_f32 v97, 0xbfb8aa3b, v130\n\t"
    "v_mul_f32 v98, 0xbfb8aa3b, v131\n\t"
    "v_exp_f32 v97, v97\n\t"
    "v_exp_f32 v98, v98\n\t"
    "v_add_f32 v97, 1.0, v97\n\t"
    "v_add_f32 v98, 1.0, v98\n\t"
    "v_rcp_f32 v97, v97\n\t"
    "v_rcp_f32 v98, v98\n\t"
    "v_mul_f32 v130, v130, v97\n\t"
    "v_mul_f32 v131, v131, v98\n\t"
    "v_cvt_pk_bf16_f32 v96, v132, v133\n\t"
    "v_lshlrev_b32 v132, 16, v96\n\t"
    "v_and_b32 v133, 0xffff0000, v96\n\t"
    "v_mul_f32 v97, 0xbfb8aa3b, v132\n\t"
    "v_mul_f32 v98, 0xbfb8aa3b, v133\n\t"
    "v_exp_f32 v97, v97\n\t"
    "v_exp_f32 v98, v98\n\t"
    "v_add_f32 v97, 1.0, v97\n\t"
    "v_add_f32 v98, 1.0, v98\n\t"
    "v_rcp_f32 v97, v97\n\t"
    "v_rcp_f32 v98, v98\n\t"
    "v_mul_f32 v132, v132, v97\n\t"
    "v_mul_f32 v133, v133, v98\n\t"
    "v_cvt_pk_bf16_f32 v96, v134, v135\n\t"
    "v_lshlrev_b32 v134, 16, v96\n\t"
    "v_and_b32 v135, 0xffff0000, v96\n\t"
    "v_mul_f32 v97, 0xbfb8aa3b, v134\n\t"
    "v_mul_f32 v98, 0xbfb8aa3b, v135\n\t"
    "v_exp_f32 v97, v97\n\t"
    "v_exp_f32 v98, v98\n\t"
    "v_add_f32 v97, 1.0, v97\n\t"
    "v_add_f32 v98, 1.0, v98\n\t"
    "v_rcp_f32 v97, v97\n\t"
    "v_rcp_f32 v98, v98\n\t"
    "v_mul_f32 v134, v134, v97\n\t"
    "v_mul_f32 v135, v135, v98\n\t"
    "v_cvt_pk_bf16_f32 v96, v136, v137\n\t"
    "v_lshlrev_b32 v136, 16, v96\n\t"
    "v_and_b32 v137, 0xffff0000, v96\n\t"
    "v_mul_f32 v97, 0xbfb8aa3b, v136\n\t"
    "v_mul_f32 v98, 0xbfb8aa3b, v137\n\t"
    "v_exp_f32 v97, v97\n\t"
    "v_exp_f32 v98, v98\n\t"
    "v_add_f32 v97, 1.0, v97\n\t"
    "v_add_f32 v98, 1.0, v98\n\t"
    "v_rcp_f32 v97, v97\n\t"
    "v_rcp_f32 v98, v98\n\t"
    "v_mul_f32 v136, v136, v97\n\t"
    "v_mul_f32 v137, v137, v98\n\t"
    "v_cvt_pk_bf16_f32 v96, v138, v139\n\t"
    "v_lshlrev_b32 v138, 16, v96\n\t"
    "v_and_b32 v139, 0xffff0000, v96\n\t"
    "v_mul_f32 v97, 0xbfb8aa3b, v138\n\t"
    "v_mul_f32 v98, 0xbfb8aa3b, v139\n\t"
    "v_exp_f32 v97, v97\n\t"
    "v_exp_f32 v98, v98\n\t"
    "v_add_f32 v97, 1.0, v97\n\t"
    "v_add_f32 v98, 1.0, v98\n\t"
    "v_rcp_f32 v97, v97\n\t"
    "v_rcp_f32 v98, v98\n\t"
    "v_mul_f32 v138, v138, v97\n\t"
    "v_mul_f32 v139, v139, v98\n\t"
    "v_cvt_pk_bf16_f32 v96, v140, v141\n\t"
    "v_lshlrev_b32 v140, 16, v96\n\t"
    "v_and_b32 v141, 0xffff0000, v96\n\t"
    "v_mul_f32 v97, 0xbfb8aa3b, v140\n\t"
    "v_mul_f32 v98, 0xbfb8aa3b, v141\n\t"
    "v_exp_f32 v97, v97\n\t"
    "v_exp_f32 v98, v98\n\t"
    "v_add_f32 v97, 1.0, v97\n\t"
    "v_add_f32 v98, 1.0, v98\n\t"
    "v_rcp_f32 v97, v97\n\t"
    "v_rcp_f32 v98, v98\n\t"
    "v_mul_f32 v140, v140, v97\n\t"
    "v_mul_f32 v141, v141, v98\n\t"
    "v_cvt_pk_bf16_f32 v96, v142, v143\n\t"
    "v_lshlrev_b32 v142, 16, v96\n\t"
    "v_and_b32 v143, 0xffff0000, v96\n\t"
    "v_mul_f32 v97, 0xbfb8aa3b, v142\n\t"
    "v_mul_f32 v98, 0xbfb8aa3b, v143\n\t"
    "v_exp_f32 v97, v97\n\t"
    "v_exp_f32 v98, v98\n\t"
    "v_add_f32 v97, 1.0, v97\n\t"
    "v_add_f32 v98, 1.0, v98\n\t"
    "v_rcp_f32 v97, v97\n\t"
    "v_rcp_f32 v98, v98\n\t"
    "v_mul_f32 v142, v142, v97\n\t"
    "v_mul_f32 v143, v143, v98\n\t"
    "v_cvt_pk_bf16_f32 v96, v144, v145\n\t"
    "v_lshlrev_b32 v144, 16, v96\n\t"
    "v_and_b32 v145, 0xffff0000, v96\n\t"
    "v_mul_f32 v97, 0xbfb8aa3b, v144\n\t"
    "v_mul_f32 v98, 0xbfb8aa3b, v145\n\t"
    "v_exp_f32 v97, v97\n\t"
    "v_exp_f32 v98, v98\n\t"
    "v_add_f32 v97, 1.0, v97\n\t"
    "v_add_f32 v98, 1.0, v98\n\t"
    "v_rcp_f32 v97, v97\n\t"
    "v_rcp_f32 v98, v98\n\t"
    "v_mul_f32 v144, v144, v97\n\t"
    "v_mul_f32 v145, v145, v98\n\t"
    "v_cvt_pk_bf16_f32 v96, v146, v147\n\t"
    "v_lshlrev_b32 v146, 16, v96\n\t"
    "v_and_b32 v147, 0xffff0000, v96\n\t"
    "v_mul_f32 v97, 0xbfb8aa3b, v146\n\t"
    "v_mul_f32 v98, 0xbfb8aa3b, v147\n\t"
    "v_exp_f32 v97, v97\n\t"
    "v_exp_f32 v98, v98\n\t"
    "v_add_f32 v97, 1.0, v97\n\t"
    "v_add_f32 v98, 1.0, v98\n\t"
    "v_rcp_f32 v97, v97\n\t"
    "v_rcp_f32 v98, v98\n\t"
    "v_mul_f32 v146, v146, v97\n\t"
    "v_mul_f32 v147, v147, v98\n\t"
    "v_cvt_pk_bf16_f32 v96, v148, v149\n\t"
    "v_lshlrev_b32 v148, 16, v96\n\t"
    "v_and_b32 v149, 0xffff0000, v96\n\t"
    "v_mul_f32 v97, 0xbfb8aa3b, v148\n\t"
    "v_mul_f32 v98, 0xbfb8aa3b, v149\n\t"
    "v_exp_f32 v97, v97\n\t"
    "v_exp_f32 v98, v98\n\t"
    "v_add_f32 v97, 1.0, v97\n\t"
    "v_add_f32 v98, 1.0, v98\n\t"
    "v_rcp_f32 v97, v97\n\t"
    "v_rcp_f32 v98, v98\n\t"
    "v_mul_f32 v148, v148, v97\n\t"
    "v_mul_f32 v149, v149, v98\n\t"
    "v_cvt_pk_bf16_f32 v96, v150, v151\n\t"
    "v_lshlrev_b32 v150, 16, v96\n\t"
    "v_and_b32 v151, 0xffff0000, v96\n\t"
    "v_mul_f32 v97, 0xbfb8aa3b, v150\n\t"
    "v_mul_f32 v98, 0xbfb8aa3b, v151\n\t"
    "v_exp_f32 v97, v97\n\t"
    "v_exp_f32 v98, v98\n\t"
    "v_add_f32 v97, 1.0, v97\n\t"
    "v_add_f32 v98, 1.0, v98\n\t"
    "v_rcp_f32 v97, v97\n\t"
    "v_rcp_f32 v98, v98\n\t"
    "v_mul_f32 v150, v150, v97\n\t"
    "v_mul_f32 v151, v151, v98\n\t"
    "v_cvt_pk_bf16_f32 v96, v152, v153\n\t"
    "v_lshlrev_b32 v152, 16, v96\n\t"
    "v_and_b32 v153, 0xffff0000, v96\n\t"
    "v_mul_f32 v97, 0xbfb8aa3b, v152\n\t"
    "v_mul_f32 v98, 0xbfb8aa3b, v153\n\t"
    "v_exp_f32 v97, v97\n\t"
    "v_exp_f32 v98, v98\n\t"
    "v_add_f32 v97, 1.0, v97\n\t"
    "v_add_f32 v98, 1.0, v98\n\t"
    "v_rcp_f32 v97, v97\n\t"
    "v_rcp_f32 v98, v98\n\t"
    "v_mul_f32 v152, v152, v97\n\t"
    "v_mul_f32 v153, v153, v98\n\t"
    "v_cvt_pk_bf16_f32 v96, v154, v155\n\t"
    "v_lshlrev_b32 v154, 16, v96\n\t"
    "v_and_b32 v155, 0xffff0000, v96\n\t"
    "v_mul_f32 v97, 0xbfb8aa3b, v154\n\t"
    "v_mul_f32 v98, 0xbfb8aa3b, v155\n\t"
    "v_exp_f32 v97, v97\n\t"
    "v_exp_f32 v98, v98\n\t"
    "v_add_f32 v97, 1.0, v97\n\t"
    "v_add_f32 v98, 1.0, v98\n\t"
    "v_rcp_f32 v97, v97\n\t"
    "v_rcp_f32 v98, v98\n\t"
    "v_mul_f32 v154, v154, v97\n\t"
    "v_mul_f32 v155, v155, v98\n\t"
    "v_cvt_pk_bf16_f32 v96, v156, v157\n\t"
    "v_lshlrev_b32 v156, 16, v96\n\t"
    "v_and_b32 v157, 0xffff0000, v96\n\t"
    "v_mul_f32 v97, 0xbfb8aa3b, v156\n\t"
    "v_mul_f32 v98, 0xbfb8aa3b, v157\n\t"
    "v_exp_f32 v97, v97\n\t"
    "v_exp_f32 v98, v98\n\t"
    "v_add_f32 v97, 1.0, v97\n\t"
    "v_add_f32 v98, 1.0, v98\n\t"
    "v_rcp_f32 v97, v97\n\t"
    "v_rcp_f32 v98, v98\n\t"
    "v_mul_f32 v156, v156, v97\n\t"
    "v_mul_f32 v157, v157, v98\n\t"
    "v_cvt_pk_bf16_f32 v96, v158, v159\n\t"
    "v_lshlrev_b32 v158, 16, v96\n\t"
    "v_and_b32 v159, 0xffff0000, v96\n\t"
    "v_mul_f32 v97, 0xbfb8aa3b, v158\n\t"
    "v_mul_f32 v98, 0xbfb8aa3b, v159\n\t"
    "v_exp_f32 v97, v97\n\t"
    "v_exp_f32 v98, v98\n\t"
    "v_add_f32 v97, 1.0, v97\n\t"
    "v_add_f32 v98, 1.0, v98\n\t"
    "v_rcp_f32 v97, v97\n\t"
    "v_rcp_f32 v98, v98\n\t"
    "v_mul_f32 v158, v158, v97\n\t"
    "v_mul_f32 v159, v159, v98\n\t"
    "v_cvt_pk_bf16_f32 v96, v160, v161\n\t"
    "v_lshlrev_b32 v160, 16, v96\n\t"
    "v_and_b32 v161, 0xffff0000, v96\n\t"
    "v_mul_f32 v97, 0xbfb8aa3b, v160\n\t"
    "v_mul_f32 v98, 0xbfb8aa3b, v161\n\t"
    "v_exp_f32 v97, v97\n\t"
    "v_exp_f32 v98, v98\n\t"
    "v_add_f32 v97, 1.0, v97\n\t"
    "v_add_f32 v98, 1.0, v98\n\t"
    "v_rcp_f32 v97, v97\n\t"
    "v_rcp_f32 v98, v98\n\t"
    "v_mul_f32 v160, v160, v97\n\t"
    "v_mul_f32 v161, v161, v98\n\t"
    "v_cvt_pk_bf16_f32 v96, v162, v163\n\t"
    "v_lshlrev_b32 v162, 16, v96\n\t"
    "v_and_b32 v163, 0xffff0000, v96\n\t"
    "v_mul_f32 v97, 0xbfb8aa3b, v162\n\t"
    "v_mul_f32 v98, 0xbfb8aa3b, v163\n\t"
    "v_exp_f32 v97, v97\n\t"
    "v_exp_f32 v98, v98\n\t"
    "v_add_f32 v97, 1.0, v97\n\t"
    "v_add_f32 v98, 1.0, v98\n\t"
    "v_rcp_f32 v97, v97\n\t"
    "v_rcp_f32 v98, v98\n\t"
    "v_mul_f32 v162, v162, v97\n\t"
    "v_mul_f32 v163, v163, v98\n\t"
    "v_cvt_pk_bf16_f32 v96, v164, v165\n\t"
    "v_lshlrev_b32 v164, 16, v96\n\t"
    "v_and_b32 v165, 0xffff0000, v96\n\t"
    "v_mul_f32 v97, 0xbfb8aa3b, v164\n\t"
    "v_mul_f32 v98, 0xbfb8aa3b, v165\n\t"
    "v_exp_f32 v97, v97\n\t"
    "v_exp_f32 v98, v98\n\t"
    "v_add_f32 v97, 1.0, v97\n\t"
    "v_add_f32 v98, 1.0, v98\n\t"
    "v_rcp_f32 v97, v97\n\t"
    "v_rcp_f32 v98, v98\n\t"
    "v_mul_f32 v164, v164, v97\n\t"
    "v_mul_f32 v165, v165, v98\n\t"
    "v_cvt_pk_bf16_f32 v96, v166, v167\n\t"
    "v_lshlrev_b32 v166, 16, v96\n\t"
    "v_and_b32 v167, 0xffff0000, v96\n\t"
    "v_mul_f32 v97, 0xbfb8aa3b, v166\n\t"
    "v_mul_f32 v98, 0xbfb8aa3b, v167\n\t"
    "v_exp_f32 v97, v97\n\t"
    "v_exp_f32 v98, v98\n\t"
    "v_add_f32 v97, 1.0, v97\n\t"
    "v_add_f32 v98, 1.0, v98\n\t"
    "v_rcp_f32 v97, v97\n\t"
    "v_rcp_f32 v98, v98\n\t"
    "v_mul_f32 v166, v166, v97\n\t"
    "v_mul_f32 v167, v167, v98\n\t"
    "v_cvt_pk_bf16_f32 v96, v168, v169\n\t"
    "v_lshlrev_b32 v168, 16, v96\n\t"
    "v_and_b32 v169, 0xffff0000, v96\n\t"
    "v_mul_f32 v97, 0xbfb8aa3b, v168\n\t"
    "v_mul_f32 v98, 0xbfb8aa3b, v169\n\t"
    "v_exp_f32 v97, v97\n\t"
    "v_exp_f32 v98, v98\n\t"
    "v_add_f32 v97, 1.0, v97\n\t"
    "v_add_f32 v98, 1.0, v98\n\t"
    "v_rcp_f32 v97, v97\n\t"
    "v_rcp_f32 v98, v98\n\t"
    "v_mul_f32 v168, v168, v97\n\t"
    "v_mul_f32 v169, v169, v98\n\t"
    "v_cvt_pk_bf16_f32 v96, v170, v171\n\t"
    "v_lshlrev_b32 v170, 16, v96\n\t"
    "v_and_b32 v171, 0xffff0000, v96\n\t"
    "v_mul_f32 v97, 0xbfb8aa3b, v170\n\t"
    "v_mul_f32 v98, 0xbfb8aa3b, v171\n\t"
    "v_exp_f32 v97, v97\n\t"
    "v_exp_f32 v98, v98\n\t"
    "v_add_f32 v97, 1.0, v97\n\t"
    "v_add_f32 v98, 1.0, v98\n\t"
    "v_rcp_f32 v97, v97\n\t"
    "v_rcp_f32 v98, v98\n\t"
    "v_mul_f32 v170, v170, v97\n\t"
    "v_mul_f32 v171, v171, v98\n\t"
    "v_cvt_pk_bf16_f32 v96, v172, v173\n\t"
    "v_lshlrev_b32 v172, 16, v96\n\t"
    "v_and_b32 v173, 0xffff0000, v96\n\t"
    "v_mul_f32 v97, 0xbfb8aa3b, v172\n\t"
    "v_mul_f32 v98, 0xbfb8aa3b, v173\n\t"
    "v_exp_f32 v97, v97\n\t"
    "v_exp_f32 v98, v98\n\t"
    "v_add_f32 v97, 1.0, v97\n\t"
    "v_add_f32 v98, 1.0, v98\n\t"
    "v_rcp_f32 v97, v97\n\t"
    "v_rcp_f32 v98, v98\n\t"
    "v_mul_f32 v172, v172, v97\n\t"
    "v_mul_f32 v173, v173, v98\n\t"
    "v_cvt_pk_bf16_f32 v96, v174, v175\n\t"
    "v_lshlrev_b32 v174, 16, v96\n\t"
    "v_and_b32 v175, 0xffff0000, v96\n\t"
    "v_mul_f32 v97, 0xbfb8aa3b, v174\n\t"
    "v_mul_f32 v98, 0xbfb8aa3b, v175\n\t"
    "v_exp_f32 v97, v97\n\t"
    "v_exp_f32 v98, v98\n\t"
    "v_add_f32 v97, 1.0, v97\n\t"
    "v_add_f32 v98, 1.0, v98\n\t"
    "v_rcp_f32 v97, v97\n\t"
    "v_rcp_f32 v98, v98\n\t"
    "v_mul_f32 v174, v174, v97\n\t"
    "v_mul_f32 v175, v175, v98\n\t"
    "v_cvt_pk_bf16_f32 v96, v176, v177\n\t"
    "v_lshlrev_b32 v176, 16, v96\n\t"
    "v_and_b32 v177, 0xffff0000, v96\n\t"
    "v_mul_f32 v97, 0xbfb8aa3b, v176\n\t"
    "v_mul_f32 v98, 0xbfb8aa3b, v177\n\t"
    "v_exp_f32 v97, v97\n\t"
    "v_exp_f32 v98, v98\n\t"
    "v_add_f32 v97, 1.0, v97\n\t"
    "v_add_f32 v98, 1.0, v98\n\t"
    "v_rcp_f32 v97, v97\n\t"
    "v_rcp_f32 v98, v98\n\t"
    "v_mul_f32 v176, v176, v97\n\t"
    "v_mul_f32 v177, v177, v98\n\t"
    "v_cvt_pk_bf16_f32 v96, v178, v179\n\t"
    "v_lshlrev_b32 v178, 16, v96\n\t"
    "v_and_b32 v179, 0xffff0000, v96\n\t"
    "v_mul_f32 v97, 0xbfb8aa3b, v178\n\t"
    "v_mul_f32 v98, 0xbfb8aa3b, v179\n\t"
    "v_exp_f32 v97, v97\n\t"
    "v_exp_f32 v98, v98\n\t"
    "v_add_f32 v97, 1.0, v97\n\t"
    "v_add_f32 v98, 1.0, v98\n\t"
    "v_rcp_f32 v97, v97\n\t"
    "v_rcp_f32 v98, v98\n\t"
    "v_mul_f32 v178, v178, v97\n\t"
    "v_mul_f32 v179, v179, v98\n\t"
    "v_cvt_pk_bf16_f32 v96, v180, v181\n\t"
    "v_lshlrev_b32 v180, 16, v96\n\t"
    "v_and_b32 v181, 0xffff0000, v96\n\t"
    "v_mul_f32 v97, 0xbfb8aa3b, v180\n\t"
    "v_mul_f32 v98, 0xbfb8aa3b, v181\n\t"
    "v_exp_f32 v97, v97\n\t"
    "v_exp_f32 v98, v98\n\t"
    "v_add_f32 v97, 1.0, v97\n\t"
    "v_add_f32 v98, 1.0, v98\n\t"
    "v_rcp_f32 v97, v97\n\t"
    "v_rcp_f32 v98, v98\n\t"
    "v_mul_f32 v180, v180, v97\n\t"
    "v_mul_f32 v181, v181, v98\n\t"
    "v_cvt_pk_bf16_f32 v96, v182, v183\n\t"
    "v_lshlrev_b32 v182, 16, v96\n\t"
    "v_and_b32 v183, 0xffff0000, v96\n\t"
    "v_mul_f32 v97, 0xbfb8aa3b, v182\n\t"
    "v_mul_f32 v98, 0xbfb8aa3b, v183\n\t"
    "v_exp_f32 v97, v97\n\t"
    "v_exp_f32 v98, v98\n\t"
    "v_add_f32 v97, 1.0, v97\n\t"
    "v_add_f32 v98, 1.0, v98\n\t"
    "v_rcp_f32 v97, v97\n\t"
    "v_rcp_f32 v98, v98\n\t"
    "v_mul_f32 v182, v182, v97\n\t"
    "v_mul_f32 v183, v183, v98\n\t"
    "v_cvt_pk_bf16_f32 v96, v184, v185\n\t"
    "v_lshlrev_b32 v184, 16, v96\n\t"
    "v_and_b32 v185, 0xffff0000, v96\n\t"
    "v_mul_f32 v97, 0xbfb8aa3b, v184\n\t"
    "v_mul_f32 v98, 0xbfb8aa3b, v185\n\t"
    "v_exp_f32 v97, v97\n\t"
    "v_exp_f32 v98, v98\n\t"
    "v_add_f32 v97, 1.0, v97\n\t"
    "v_add_f32 v98, 1.0, v98\n\t"
    "v_rcp_f32 v97, v97\n\t"
    "v_rcp_f32 v98, v98\n\t"
    "v_mul_f32 v184, v184, v97\n\t"
    "v_mul_f32 v185, v185, v98\n\t"
    "v_cvt_pk_bf16_f32 v96, v186, v187\n\t"
    "v_lshlrev_b32 v186, 16, v96\n\t"
    "v_and_b32 v187, 0xffff0000, v96\n\t"
    "v_mul_f32 v97, 0xbfb8aa3b, v186\n\t"
    "v_mul_f32 v98, 0xbfb8aa3b, v187\n\t"
    "v_exp_f32 v97, v97\n\t"
    "v_exp_f32 v98, v98\n\t"
    "v_add_f32 v97, 1.0, v97\n\t"
    "v_add_f32 v98, 1.0, v98\n\t"
    "v_rcp_f32 v97, v97\n\t"
    "v_rcp_f32 v98, v98\n\t"
    "v_mul_f32 v186, v186, v97\n\t"
    "v_mul_f32 v187, v187, v98\n\t"
    "v_cvt_pk_bf16_f32 v96, v188, v189\n\t"
    "v_lshlrev_b32 v188, 16, v96\n\t"
    "v_and_b32 v189, 0xffff0000, v96\n\t"
    "v_mul_f32 v97, 0xbfb8aa3b, v188\n\t"
    "v_mul_f32 v98, 0xbfb8aa3b, v189\n\t"
    "v_exp_f32 v97, v97\n\t"
    "v_exp_f32 v98, v98\n\t"
    "v_add_f32 v97, 1.0, v97\n\t"
    "v_add_f32 v98, 1.0, v98\n\t"
    "v_rcp_f32 v97, v97\n\t"
    "v_rcp_f32 v98, v98\n\t"
    "v_mul_f32 v188, v188, v97\n\t"
    "v_mul_f32 v189, v189, v98\n\t"
    "v_cvt_pk_bf16_f32 v96, v190, v191\n\t"
    "v_lshlrev_b32 v190, 16, v96\n\t"
    "v_and_b32 v191, 0xffff0000, v96\n\t"
    "v_mul_f32 v97, 0xbfb8aa3b, v190\n\t"
    "v_mul_f32 v98, 0xbfb8aa3b, v191\n\t"
    "v_exp_f32 v97, v97\n\t"
    "v_exp_f32 v98, v98\n\t"
    "v_add_f32 v97, 1.0, v97\n\t"
    "v_add_f32 v98, 1.0, v98\n\t"
    "v_rcp_f32 v97, v97\n\t"
    "v_rcp_f32 v98, v98\n\t"
    "v_mul_f32 v190, v190, v97\n\t"
    "v_mul_f32 v191, v191, v98\n\t"
    "v_cvt_pk_bf16_f32 v96, v192, v193\n\t"
    "v_lshlrev_b32 v192, 16, v96\n\t"
    "v_and_b32 v193, 0xffff0000, v96\n\t"
    "v_mul_f32 v97, 0xbfb8aa3b, v192\n\t"
    "v_mul_f32 v98, 0xbfb8aa3b, v193\n\t"
    "v_exp_f32 v97, v97\n\t"
    "v_exp_f32 v98, v98\n\t"
    "v_add_f32 v97, 1.0, v97\n\t"
    "v_add_f32 v98, 1.0, v98\n\t"
    "v_rcp_f32 v97, v97\n\t"
    "v_rcp_f32 v98, v98\n\t"
    "v_mul_f32 v192, v192, v97\n\t"
    "v_mul_f32 v193, v193, v98\n\t"
    "v_cvt_pk_bf16_f32 v96, v194, v195\n\t"
    "v_lshlrev_b32 v194, 16, v96\n\t"
    "v_and_b32 v195, 0xffff0000, v96\n\t"
    "v_mul_f32 v97, 0xbfb8aa3b, v194\n\t"
    "v_mul_f32 v98, 0xbfb8aa3b, v195\n\t"
    "v_exp_f32 v97, v97\n\t"
    "v_exp_f32 v98, v98\n\t"
    "v_add_f32 v97, 1.0, v97\n\t"
    "v_add_f32 v98, 1.0, v98\n\t"
    "v_rcp_f32 v97, v97\n\t"
    "v_rcp_f32 v98, v98\n\t"
    "v_mul_f32 v194, v194, v97\n\t"
    "v_mul_f32 v195, v195, v98\n\t"
    "v_cvt_pk_bf16_f32 v96, v196, v197\n\t"
    "v_lshlrev_b32 v196, 16, v96\n\t"
    "v_and_b32 v197, 0xffff0000, v96\n\t"
    "v_mul_f32 v97, 0xbfb8aa3b, v196\n\t"
    "v_mul_f32 v98, 0xbfb8aa3b, v197\n\t"
    "v_exp_f32 v97, v97\n\t"
    "v_exp_f32 v98, v98\n\t"
    "v_add_f32 v97, 1.0, v97\n\t"
    "v_add_f32 v98, 1.0, v98\n\t"
    "v_rcp_f32 v97, v97\n\t"
    "v_rcp_f32 v98, v98\n\t"
    "v_mul_f32 v196, v196, v97\n\t"
    "v_mul_f32 v197, v197, v98\n\t"
    "v_cvt_pk_bf16_f32 v96, v198, v199\n\t"
    "v_lshlrev_b32 v198, 16, v96\n\t"
    "v_and_b32 v199, 0xffff0000, v96\n\t"
    "v_mul_f32 v97, 0xbfb8aa3b, v198\n\t"
    "v_mul_f32 v98, 0xbfb8aa3b, v199\n\t"
    "v_exp_f32 v97, v97\n\t"
    "v_exp_f32 v98, v98\n\t"
    "v_add_f32 v97, 1.0, v97\n\t"
    "v_add_f32 v98, 1.0, v98\n\t"
    "v_rcp_f32 v97, v97\n\t"
    "v_rcp_f32 v98, v98\n\t"
    "v_mul_f32 v198, v198, v97\n\t"
    "v_mul_f32 v199, v199, v98\n\t"
    "v_cvt_pk_bf16_f32 v96, v200, v201\n\t"
    "v_lshlrev_b32 v200, 16, v96\n\t"
    "v_and_b32 v201, 0xffff0000, v96\n\t"
    "v_mul_f32 v97, 0xbfb8aa3b, v200\n\t"
    "v_mul_f32 v98, 0xbfb8aa3b, v201\n\t"
    "v_exp_f32 v97, v97\n\t"
    "v_exp_f32 v98, v98\n\t"
    "v_add_f32 v97, 1.0, v97\n\t"
    "v_add_f32 v98, 1.0, v98\n\t"
    "v_rcp_f32 v97, v97\n\t"
    "v_rcp_f32 v98, v98\n\t"
    "v_mul_f32 v200, v200, v97\n\t"
    "v_mul_f32 v201, v201, v98\n\t"
    "v_cvt_pk_bf16_f32 v96, v202, v203\n\t"
    "v_lshlrev_b32 v202, 16, v96\n\t"
    "v_and_b32 v203, 0xffff0000, v96\n\t"
    "v_mul_f32 v97, 0xbfb8aa3b, v202\n\t"
    "v_mul_f32 v98, 0xbfb8aa3b, v203\n\t"
    "v_exp_f32 v97, v97\n\t"
    "v_exp_f32 v98, v98\n\t"
    "v_add_f32 v97, 1.0, v97\n\t"
    "v_add_f32 v98, 1.0, v98\n\t"
    "v_rcp_f32 v97, v97\n\t"
    "v_rcp_f32 v98, v98\n\t"
    "v_mul_f32 v202, v202, v97\n\t"
    "v_mul_f32 v203, v203, v98\n\t"
    "v_cvt_pk_bf16_f32 v96, v204, v205\n\t"
    "v_lshlrev_b32 v204, 16, v96\n\t"
    "v_and_b32 v205, 0xffff0000, v96\n\t"
    "v_mul_f32 v97, 0xbfb8aa3b, v204\n\t"
    "v_mul_f32 v98, 0xbfb8aa3b, v205\n\t"
    "v_exp_f32 v97, v97\n\t"
    "v_exp_f32 v98, v98\n\t"
    "v_add_f32 v97, 1.0, v97\n\t"
    "v_add_f32 v98, 1.0, v98\n\t"
    "v_rcp_f32 v97, v97\n\t"
    "v_rcp_f32 v98, v98\n\t"
    "v_mul_f32 v204, v204, v97\n\t"
    "v_mul_f32 v205, v205, v98\n\t"
    "v_cvt_pk_bf16_f32 v96, v206, v207\n\t"
    "v_lshlrev_b32 v206, 16, v96\n\t"
    "v_and_b32 v207, 0xffff0000, v96\n\t"
    "v_mul_f32 v97, 0xbfb8aa3b, v206\n\t"
    "v_mul_f32 v98, 0xbfb8aa3b, v207\n\t"
    "v_exp_f32 v97, v97\n\t"
    "v_exp_f32 v98, v98\n\t"
    "v_add_f32 v97, 1.0, v97\n\t"
    "v_add_f32 v98, 1.0, v98\n\t"
    "v_rcp_f32 v97, v97\n\t"
    "v_rcp_f32 v98, v98\n\t"
    "v_mul_f32 v206, v206, v97\n\t"
    "v_mul_f32 v207, v207, v98\n\t"
    "v_cvt_pk_bf16_f32 v96, v208, v209\n\t"
    "v_lshlrev_b32 v208, 16, v96\n\t"
    "v_and_b32 v209, 0xffff0000, v96\n\t"
    "v_mul_f32 v97, 0xbfb8aa3b, v208\n\t"
    "v_mul_f32 v98, 0xbfb8aa3b, v209\n\t"
    "v_exp_f32 v97, v97\n\t"
    "v_exp_f32 v98, v98\n\t"
    "v_add_f32 v97, 1.0, v97\n\t"
    "v_add_f32 v98, 1.0, v98\n\t"
    "v_rcp_f32 v97, v97\n\t"
    "v_rcp_f32 v98, v98\n\t"
    "v_mul_f32 v208, v208, v97\n\t"
    "v_mul_f32 v209, v209, v98\n\t"
    "v_cvt_pk_bf16_f32 v96, v210, v211\n\t"
    "v_lshlrev_b32 v210, 16, v96\n\t"
    "v_and_b32 v211, 0xffff0000, v96\n\t"
    "v_mul_f32 v97, 0xbfb8aa3b, v210\n\t"
    "v_mul_f32 v98, 0xbfb8aa3b, v211\n\t"
    "v_exp_f32 v97, v97\n\t"
    "v_exp_f32 v98, v98\n\t"
    "v_add_f32 v97, 1.0, v97\n\t"
    "v_add_f32 v98, 1.0, v98\n\t"
    "v_rcp_f32 v97, v97\n\t"
    "v_rcp_f32 v98, v98\n\t"
    "v_mul_f32 v210, v210, v97\n\t"
    "v_mul_f32 v211, v211, v98\n\t"
    "v_cvt_pk_bf16_f32 v96, v212, v213\n\t"
    "v_lshlrev_b32 v212, 16, v96\n\t"
    "v_and_b32 v213, 0xffff0000, v96\n\t"
    "v_mul_f32 v97, 0xbfb8aa3b, v212\n\t"
    "v_mul_f32 v98, 0xbfb8aa3b, v213\n\t"
    "v_exp_f32 v97, v97\n\t"
    "v_exp_f32 v98, v98\n\t"
    "v_add_f32 v97, 1.0, v97\n\t"
    "v_add_f32 v98, 1.0, v98\n\t"
    "v_rcp_f32 v97, v97\n\t"
    "v_rcp_f32 v98, v98\n\t"
    "v_mul_f32 v212, v212, v97\n\t"
    "v_mul_f32 v213, v213, v98\n\t"
    "v_cvt_pk_bf16_f32 v96, v214, v215\n\t"
    "v_lshlrev_b32 v214, 16, v96\n\t"
    "v_and_b32 v215, 0xffff0000, v96\n\t"
    "v_mul_f32 v97, 0xbfb8aa3b, v214\n\t"
    "v_mul_f32 v98, 0xbfb8aa3b, v215\n\t"
    "v_exp_f32 v97, v97\n\t"
    "v_exp_f32 v98, v98\n\t"
    "v_add_f32 v97, 1.0, v97\n\t"
    "v_add_f32 v98, 1.0, v98\n\t"
    "v_rcp_f32 v97, v97\n\t"
    "v_rcp_f32 v98, v98\n\t"
    "v_mul_f32 v214, v214, v97\n\t"
    "v_mul_f32 v215, v215, v98\n\t"
    "v_cvt_pk_bf16_f32 v96, v216, v217\n\t"
    "v_lshlrev_b32 v216, 16, v96\n\t"
    "v_and_b32 v217, 0xffff0000, v96\n\t"
    "v_mul_f32 v97, 0xbfb8aa3b, v216\n\t"
    "v_mul_f32 v98, 0xbfb8aa3b, v217\n\t"
    "v_exp_f32 v97, v97\n\t"
    "v_exp_f32 v98, v98\n\t"
    "v_add_f32 v97, 1.0, v97\n\t"
    "v_add_f32 v98, 1.0, v98\n\t"
    "v_rcp_f32 v97, v97\n\t"
    "v_rcp_f32 v98, v98\n\t"
    "v_mul_f32 v216, v216, v97\n\t"
    "v_mul_f32 v217, v217, v98\n\t"
    "v_cvt_pk_bf16_f32 v96, v218, v219\n\t"
    "v_lshlrev_b32 v218, 16, v96\n\t"
    "v_and_b32 v219, 0xffff0000, v96\n\t"
    "v_mul_f32 v97, 0xbfb8aa3b, v218\n\t"
    "v_mul_f32 v98, 0xbfb8aa3b, v219\n\t"
    "v_exp_f32 v97, v97\n\t"
    "v_exp_f32 v98, v98\n\t"
    "v_add_f32 v97, 1.0, v97\n\t"
    "v_add_f32 v98, 1.0, v98\n\t"
    "v_rcp_f32 v97, v97\n\t"
    "v_rcp_f32 v98, v98\n\t"
    "v_mul_f32 v218, v218, v97\n\t"
    "v_mul_f32 v219, v219, v98\n\t"
    "v_cvt_pk_bf16_f32 v96, v220, v221\n\t"
    "v_lshlrev_b32 v220, 16, v96\n\t"
    "v_and_b32 v221, 0xffff0000, v96\n\t"
    "v_mul_f32 v97, 0xbfb8aa3b, v220\n\t"
    "v_mul_f32 v98, 0xbfb8aa3b, v221\n\t"
    "v_exp_f32 v97, v97\n\t"
    "v_exp_f32 v98, v98\n\t"
    "v_add_f32 v97, 1.0, v97\n\t"
    "v_add_f32 v98, 1.0, v98\n\t"
    "v_rcp_f32 v97, v97\n\t"
    "v_rcp_f32 v98, v98\n\t"
    "v_mul_f32 v220, v220, v97\n\t"
    "v_mul_f32 v221, v221, v98\n\t"
    "v_cvt_pk_bf16_f32 v96, v222, v223\n\t"
    "v_lshlrev_b32 v222, 16, v96\n\t"
    "v_and_b32 v223, 0xffff0000, v96\n\t"
    "v_mul_f32 v97, 0xbfb8aa3b, v222\n\t"
    "v_mul_f32 v98, 0xbfb8aa3b, v223\n\t"
    "v_exp_f32 v97, v97\n\t"
    "v_exp_f32 v98, v98\n\t"
    "v_add_f32 v97, 1.0, v97\n\t"
    "v_add_f32 v98, 1.0, v98\n\t"
    "v_rcp_f32 v97, v97\n\t"
    "v_rcp_f32 v98, v98\n\t"
    "v_mul_f32 v222, v222, v97\n\t"
    "v_mul_f32 v223, v223, v98\n\t"
    "v_cvt_pk_bf16_f32 v96, v224, v225\n\t"
    "v_lshlrev_b32 v224, 16, v96\n\t"
    "v_and_b32 v225, 0xffff0000, v96\n\t"
    "v_mul_f32 v97, 0xbfb8aa3b, v224\n\t"
    "v_mul_f32 v98, 0xbfb8aa3b, v225\n\t"
    "v_exp_f32 v97, v97\n\t"
    "v_exp_f32 v98, v98\n\t"
    "v_add_f32 v97, 1.0, v97\n\t"
    "v_add_f32 v98, 1.0, v98\n\t"
    "v_rcp_f32 v97, v97\n\t"
    "v_rcp_f32 v98, v98\n\t"
    "v_mul_f32 v224, v224, v97\n\t"
    "v_mul_f32 v225, v225, v98\n\t"
    "v_cvt_pk_bf16_f32 v96, v226, v227\n\t"
    "v_lshlrev_b32 v226, 16, v96\n\t"
    "v_and_b32 v227, 0xffff0000, v96\n\t"
    "v_mul_f32 v97, 0xbfb8aa3b, v226\n\t"
    "v_mul_f32 v98, 0xbfb8aa3b, v227\n\t"
    "v_exp_f32 v97, v97\n\t"
    "v_exp_f32 v98, v98\n\t"
    "v_add_f32 v97, 1.0, v97\n\t"
    "v_add_f32 v98, 1.0, v98\n\t"
    "v_rcp_f32 v97, v97\n\t"
    "v_rcp_f32 v98, v98\n\t"
    "v_mul_f32 v226, v226, v97\n\t"
    "v_mul_f32 v227, v227, v98\n\t"
    "v_cvt_pk_bf16_f32 v96, v228, v229\n\t"
    "v_lshlrev_b32 v228, 16, v96\n\t"
    "v_and_b32 v229, 0xffff0000, v96\n\t"
    "v_mul_f32 v97, 0xbfb8aa3b, v228\n\t"
    "v_mul_f32 v98, 0xbfb8aa3b, v229\n\t"
    "v_exp_f32 v97, v97\n\t"
    "v_exp_f32 v98, v98\n\t"
    "v_add_f32 v97, 1.0, v97\n\t"
    "v_add_f32 v98, 1.0, v98\n\t"
    "v_rcp_f32 v97, v97\n\t"
    "v_rcp_f32 v98, v98\n\t"
    "v_mul_f32 v228, v228, v97\n\t"
    "v_mul_f32 v229, v229, v98\n\t"
    "v_cvt_pk_bf16_f32 v96, v230, v231\n\t"
    "v_lshlrev_b32 v230, 16, v96\n\t"
    "v_and_b32 v231, 0xffff0000, v96\n\t"
    "v_mul_f32 v97, 0xbfb8aa3b, v230\n\t"
    "v_mul_f32 v98, 0xbfb8aa3b, v231\n\t"
    "v_exp_f32 v97, v97\n\t"
    "v_exp_f32 v98, v98\n\t"
    "v_add_f32 v97, 1.0, v97\n\t"
    "v_add_f32 v98, 1.0, v98\n\t"
    "v_rcp_f32 v97, v97\n\t"
    "v_rcp_f32 v98, v98\n\t"
    "v_mul_f32 v230, v230, v97\n\t"
    "v_mul_f32 v231, v231, v98\n\t"
    "v_cvt_pk_bf16_f32 v96, v232, v233\n\t"
    "v_lshlrev_b32 v232, 16, v96\n\t"
    "v_and_b32 v233, 0xffff0000, v96\n\t"
    "v_mul_f32 v97, 0xbfb8aa3b, v232\n\t"
    "v_mul_f32 v98, 0xbfb8aa3b, v233\n\t"
    "v_exp_f32 v97, v97\n\t"
    "v_exp_f32 v98, v98\n\t"
    "v_add_f32 v97, 1.0, v97\n\t"
    "v_add_f32 v98, 1.0, v98\n\t"
    "v_rcp_f32 v97, v97\n\t"
    "v_rcp_f32 v98, v98\n\t"
    "v_mul_f32 v232, v232, v97\n\t"
    "v_mul_f32 v233, v233, v98\n\t"
    "v_cvt_pk_bf16_f32 v96, v234, v235\n\t"
    "v_lshlrev_b32 v234, 16, v96\n\t"
    "v_and_b32 v235, 0xffff0000, v96\n\t"
    "v_mul_f32 v97, 0xbfb8aa3b, v234\n\t"
    "v_mul_f32 v98, 0xbfb8aa3b, v235\n\t"
    "v_exp_f32 v97, v97\n\t"
    "v_exp_f32 v98, v98\n\t"
    "v_add_f32 v97, 1.0, v97\n\t"
    "v_add_f32 v98, 1.0, v98\n\t"
    "v_rcp_f32 v97, v97\n\t"
    "v_rcp_f32 v98, v98\n\t"
    "v_mul_f32 v234, v234, v97\n\t"
    "v_mul_f32 v235, v235, v98\n\t"
    "v_cvt_pk_bf16_f32 v96, v236, v237\n\t"
    "v_lshlrev_b32 v236, 16, v96\n\t"
    "v_and_b32 v237, 0xffff0000, v96\n\t"
    "v_mul_f32 v97, 0xbfb8aa3b, v236\n\t"
    "v_mul_f32 v98, 0xbfb8aa3b, v237\n\t"
    "v_exp_f32 v97, v97\n\t"
    "v_exp_f32 v98, v98\n\t"
    "v_add_f32 v97, 1.0, v97\n\t"
    "v_add_f32 v98, 1.0, v98\n\t"
    "v_rcp_f32 v97, v97\n\t"
    "v_rcp_f32 v98, v98\n\t"
    "v_mul_f32 v236, v236, v97\n\t"
    "v_mul_f32 v237, v237, v98\n\t"
    "v_cvt_pk_bf16_f32 v96, v238, v239\n\t"
    "v_lshlrev_b32 v238, 16, v96\n\t"
    "v_and_b32 v239, 0xffff0000, v96\n\t"
    "v_mul_f32 v97, 0xbfb8aa3b, v238\n\t"
    "v_mul_f32 v98, 0xbfb8aa3b, v239\n\t"
    "v_exp_f32 v97, v97\n\t"
    "v_exp_f32 v98, v98\n\t"
    "v_add_f32 v97, 1.0, v97\n\t"
    "v_add_f32 v98, 1.0, v98\n\t"
    "v_rcp_f32 v97, v97\n\t"
    "v_rcp_f32 v98, v98\n\t"
    "v_mul_f32 v238, v238, v97\n\t"
    "v_mul_f32 v239, v239, v98\n\t"
    "v_cvt_pk_bf16_f32 v96, v240, v241\n\t"
    "v_lshlrev_b32 v240, 16, v96\n\t"
    "v_and_b32 v241, 0xffff0000, v96\n\t"
    "v_mul_f32 v97, 0xbfb8aa3b, v240\n\t"
    "v_mul_f32 v98, 0xbfb8aa3b, v241\n\t"
    "v_exp_f32 v97, v97\n\t"
    "v_exp_f32 v98, v98\n\t"
    "v_add_f32 v97, 1.0, v97\n\t"
    "v_add_f32 v98, 1.0, v98\n\t"
    "v_rcp_f32 v97, v97\n\t"
    "v_rcp_f32 v98, v98\n\t"
    "v_mul_f32 v240, v240, v97\n\t"
    "v_mul_f32 v241, v241, v98\n\t"
    "v_cvt_pk_bf16_f32 v96, v242, v243\n\t"
    "v_lshlrev_b32 v242, 16, v96\n\t"
    "v_and_b32 v243, 0xffff0000, v96\n\t"
    "v_mul_f32 v97, 0xbfb8aa3b, v242\n\t"
    "v_mul_f32 v98, 0xbfb8aa3b, v243\n\t"
    "v_exp_f32 v97, v97\n\t"
    "v_exp_f32 v98, v98\n\t"
    "v_add_f32 v97, 1.0, v97\n\t"
    "v_add_f32 v98, 1.0, v98\n\t"
    "v_rcp_f32 v97, v97\n\t"
    "v_rcp_f32 v98, v98\n\t"
    "v_mul_f32 v242, v242, v97\n\t"
    "v_mul_f32 v243, v243, v98\n\t"
    "v_cvt_pk_bf16_f32 v96, v244, v245\n\t"
    "v_lshlrev_b32 v244, 16, v96\n\t"
    "v_and_b32 v245, 0xffff0000, v96\n\t"
    "v_mul_f32 v97, 0xbfb8aa3b, v244\n\t"
    "v_mul_f32 v98, 0xbfb8aa3b, v245\n\t"
    "v_exp_f32 v97, v97\n\t"
    "v_exp_f32 v98, v98\n\t"
    "v_add_f32 v97, 1.0, v97\n\t"
    "v_add_f32 v98, 1.0, v98\n\t"
    "v_rcp_f32 v97, v97\n\t"
    "v_rcp_f32 v98, v98\n\t"
    "v_mul_f32 v244, v244, v97\n\t"
    "v_mul_f32 v245, v245, v98\n\t"
    "v_cvt_pk_bf16_f32 v96, v246, v247\n\t"
    "v_lshlrev_b32 v246, 16, v96\n\t"
    "v_and_b32 v247, 0xffff0000, v96\n\t"
    "v_mul_f32 v97, 0xbfb8aa3b, v246\n\t"
    "v_mul_f32 v98, 0xbfb8aa3b, v247\n\t"
    "v_exp_f32 v97, v97\n\t"
    "v_exp_f32 v98, v98\n\t"
    "v_add_f32 v97, 1.0, v97\n\t"
    "v_add_f32 v98, 1.0, v98\n\t"
    "v_rcp_f32 v97, v97\n\t"
    "v_rcp_f32 v98, v98\n\t"
    "v_mul_f32 v246, v246, v97\n\t"
    "v_mul_f32 v247, v247, v98\n\t"
    "v_cvt_pk_bf16_f32 v96, v248, v249\n\t"
    "v_lshlrev_b32 v248, 16, v96\n\t"
    "v_and_b32 v249, 0xffff0000, v96\n\t"
    "v_mul_f32 v97, 0xbfb8aa3b, v248\n\t"
    "v_mul_f32 v98, 0xbfb8aa3b, v249\n\t"
    "v_exp_f32 v97, v97\n\t"
    "v_exp_f32 v98, v98\n\t"
    "v_add_f32 v97, 1.0, v97\n\t"
    "v_add_f32 v98, 1.0, v98\n\t"
    "v_rcp_f32 v97, v97\n\t"
    "v_rcp_f32 v98, v98\n\t"
    "v_mul_f32 v248, v248, v97\n\t"
    "v_mul_f32 v249, v249, v98\n\t"
    "v_cvt_pk_bf16_f32 v96, v250, v251\n\t"
    "v_lshlrev_b32 v250, 16, v96\n\t"
    "v_and_b32 v251, 0xffff0000, v96\n\t"
    "v_mul_f32 v97, 0xbfb8aa3b, v250\n\t"
    "v_mul_f32 v98, 0xbfb8aa3b, v251\n\t"
    "v_exp_f32 v97, v97\n\t"
    "v_exp_f32 v98, v98\n\t"
    "v_add_f32 v97, 1.0, v97\n\t"
    "v_add_f32 v98, 1.0, v98\n\t"
    "v_rcp_f32 v97, v97\n\t"
    "v_rcp_f32 v98, v98\n\t"
    "v_mul_f32 v250, v250, v97\n\t"
    "v_mul_f32 v251, v251, v98\n\t"
    "v_cvt_pk_bf16_f32 v96, v252, v253\n\t"
    "v_lshlrev_b32 v252, 16, v96\n\t"
    "v_and_b32 v253, 0xffff0000, v96\n\t"
    "v_mul_f32 v97, 0xbfb8aa3b, v252\n\t"
    "v_mul_f32 v98, 0xbfb8aa3b, v253\n\t"
    "v_exp_f32 v97, v97\n\t"
    "v_exp_f32 v98, v98\n\t"
    "v_add_f32 v97, 1.0, v97\n\t"
    "v_add_f32 v98, 1.0, v98\n\t"
    "v_rcp_f32 v97, v97\n\t"
    "v_rcp_f32 v98, v98\n\t"
    "v_mul_f32 v252, v252, v97\n\t"
    "v_mul_f32 v253, v253, v98\n\t"
    "v_cvt_pk_bf16_f32 v96, v254, v255\n\t"
    "v_lshlrev_b32 v254, 16, v96\n\t"
    "v_and_b32 v255, 0xffff0000, v96\n\t"
    "v_mul_f32 v97, 0xbfb8aa3b, v254\n\t"
    "v_mul_f32 v98, 0xbfb8aa3b, v255\n\t"
    "v_exp_f32 v97, v97\n\t"
    "v_exp_f32 v98, v98\n\t"
    "v_add_f32 v97, 1.0, v97\n\t"
    "v_add_f32 v98, 1.0, v98\n\t"
    "v_rcp_f32 v97, v97\n\t"
    "v_rcp_f32 v98, v98\n\t"
    "v_mul_f32 v254, v254, v97\n\t"
    "v_mul_f32 v255, v255, v98\n\t"
    "3:\n\t"
    "s_nop 7\n\t"
    "v_cvt_pk_bf16_f32 v96, v128, v129\n\t"
    "v_cvt_pk_bf16_f32 v97, v130, v131\n\t"
    "v_cvt_pk_bf16_f32 v98, v132, v133\n\t"
    "v_cvt_pk_bf16_f32 v99, v134, v135\n\t"
    "buffer_store_dwordx4 v[96:99], v8, %[rd], s43 offen offset:0 sc1\n\t"
    "v_cvt_pk_bf16_f32 v100, v136, v137\n\t"
    "v_cvt_pk_bf16_f32 v101, v138, v139\n\t"
    "v_cvt_pk_bf16_f32 v102, v140, v141\n\t"
    "v_cvt_pk_bf16_f32 v103, v142, v143\n\t"
    "buffer_store_dwordx4 v[100:103], v8, %[rd], s43 offen offset:1024 sc1\n\t"
    "s_add_u32 s45, s43, %[s1]\n\t"
    "v_cvt_pk_bf16_f32 v104, v144, v145\n\t"
    "v_cvt_pk_bf16_f32 v105, v146, v147\n\t"
    "v_cvt_pk_bf16_f32 v106, v148, v149\n\t"
    "v_cvt_pk_bf16_f32 v107, v150, v151\n\t"
    "buffer_store_dwordx4 v[104:107], v8, %[rd], s45 offen offset:0 sc1\n\t"
    "v_cvt_pk_bf16_f32 v108, v152, v153\n\t"
    "v_cvt_pk_bf16_f32 v109, v154, v155\n\t"
    "v_cvt_pk_bf16_f32 v110, v156, v157\n\t"
    "v_cvt_pk_bf16_f32 v111, v158, v159\n\t"
    "buffer_store_dwordx4 v[108:111], v8, %[rd], s45 offen offset:1024 sc1\n\t"
    "s_add_u32 s45, s45, %[s2]\n\t"
    "v_cvt_pk_bf16_f32 v96, v160, v161\n\t"
    "v_cvt_pk_bf16_f32 v97, v162, v163\n\t"
    "v_cvt_pk_bf16_f32 v98, v164, v165\n\t"
    "v_cvt_pk_bf16_f32 v99, v166, v167\n\t"
    "buffer_store_dwordx4 v[96:99], v8, %[rd], s45 offen offset:0 sc1\n\t"
    "v_cvt_pk_bf16_f32 v100, v168, v169\n\t"
    "v_cvt_pk_bf16_f32 v101, v170, v171\n\t"
    "v_cvt_pk_bf16_f32 v102, v172, v173\n\t"
    "v_cvt_pk_bf16_f32 v103, v174, v175\n\t"
    "buffer_store_dwordx4 v[100:103], v8, %[rd], s45 offen offset:1024 sc1\n\t"
    "s_add_u32 s45, s45, %[s1]\n\t"
    "v_cvt_pk_bf16_f32 v104, v176, v177\n\t"
    "v_cvt_pk_bf16_f32 v105, v178, v179\n\t"
    "v_cvt_pk_bf16_f32 v106, v180, v181\n\t"
    "v_cvt_pk_bf16_f32 v107, v182, v183\n\t"
    "buffer_store_dwordx4 v[104:107], v8, %[rd], s45 offen offset:0 sc1\n\t"
    "v_cvt_pk_bf16_f32 v108, v184, v185\n\t"
    "v_cvt_pk_bf16_f32 v109, v186, v187\n\t"
    "v_cvt_pk_bf16_f32 v110, v188, v189\n\t"
    "v_cvt_pk_bf16_f32 v111, v190, v191\n\t"
    "buffer_store_dwordx4 v[108:111], v8, %[rd], s45 offen offset:1024 sc1\n\t"
    "s_add_u32 s45, s45, %[s2]\n\t"
    "v_cvt_pk_bf16_f32 v96, v192, v193\n\t"
    "v_cvt_pk_bf16_f32 v97, v194, v195\n\t"
    "v_cvt_pk_bf16_f32 v98, v196, v197\n\t"
    "v_cvt_pk_bf16_f32 v99, v198, v199\n\t"
    "buffer_store_dwordx4 v[96:99], v8, %[rd], s45 offen offset:0 sc1\n\t"
    "v_cvt_pk_bf16_f32 v100, v200, v201\n\t"
    "v_cvt_pk_bf16_f32 v101, v202, v203\n\t"
    "v_cvt_pk_bf16_f32 v102, v204, v205\n\t"
    "v_cvt_pk_bf16_f32 v103, v206, v207\n\t"
    "buffer_store_dwordx4 v[100:103], v8, %[rd], s45 offen offset:1024 sc1\n\t"
    "s_add_u32 s45, s45, %[s1]\n\t"
    "v_cvt_pk_bf16_f32 v104, v208, v209\n\t"
    "v_cvt_pk_bf16_f32 v105, v210, v211\n\t"
    "v_cvt_pk_bf16_f32 v106, v212, v213\n\t"
    "v_cvt_pk_bf16_f32 v107, v214, v215\n\t"
    "buffer_store_dwordx4 v[104:107], v8, %[rd], s45 offen offset:0 sc1\n\t"
    "v_cvt_pk_bf16_f32 v108, v216, v217\n\t"
    "v_cvt_pk_bf16_f32 v109, v218, v219\n\t"
    "v_cvt_pk_bf16_f32 v110, v220, v221\n\t"
    "v_cvt_pk_bf16_f32 v111, v222, v223\n\t"
    "buffer_store_dwordx4 v[108:111], v8, %[rd], s45 offen offset:1024 sc1\n\t"
    "s_add_u32 s45, s45, %[s2]\n\t"
    "v_cvt_pk_bf16_f32 v96, v224, v225\n\t"
    "v_cvt_pk_bf16_f32 v97, v226, v227\n\t"
    "v_cvt_pk_bf16_f32 v98, v228, v229\n\t"
    "v_cvt_pk_bf16_f32 v99, v230, v231\n\t"
    "buffer_store_dwordx4 v[96:99], v8, %[rd], s45 offen offset:0 sc1\n\t"
    "v_cvt_pk_bf16_f32 v100, v232, v233\n\t"
    "v_cvt_pk_bf16_f32 v101, v234, v235\n\t"
    "v_cvt_pk_bf16_f32 v102, v236, v237\n\t"
    "v_cvt_pk_bf16_f32 v103, v238, v239\n\t"
    "buffer_store_dwordx4 v[100:103], v8, %[rd], s45 offen offset:1024 sc1\n\t"
    "s_add_u32 s45, s45, %[s1]\n\t"
    "v_cvt_pk_bf16_f32 v104, v240, v241\n\t"
    "v_cvt_pk_bf16_f32 v105, v242, v243\n\t"
    "v_cvt_pk_bf16_f32 v106, v244, v245\n\t"
    "v_cvt_pk_bf16_f32 v107, v246, v247\n\t"
    "buffer_store_dwordx4 v[104:107], v8, %[rd], s45 offen offset:0 sc1\n\t"
    "v_cvt_pk_bf16_f32 v108, v248, v249\n\t"
    "v_cvt_pk_bf16_f32 v109, v250, v251\n\t"
    "v_cvt_pk_bf16_f32 v110, v252, v253\n\t"
    "v_cvt_pk_bf16_f32 v111, v254, v255\n\t"
    "buffer_store_dwordx4 v[108:111], v8, %[rd], s45 offen offset:1024 sc1\n\t"
    "s_mov_b32 s44, s47\n\t"
    "s_add_u32 s47, s47, %[fs]\n\t"
    "s_add_u32 s43, s43, %[dt]\n\t"
    "s_sub_u32 s40, s40, 1\n\t"
    "s_cmp_lg_u32 s40, 0\n\t"
    "s_cbranch_scc1 1b\n\t"
    "s_waitcnt vmcnt(0) lgkmcnt(0)\n\t"
    "s_barrier\n\t"
    "s_cmp_lg_u32 %[w], 0\n\t"
    "s_cbranch_scc1 2f\n\t"
    "s_mov_b64 exec, 1\n\t"
    "buffer_store_dword v9, off, %[rc], s44 sc1\n\t"
    "s_mov_b64 exec, -1\n\t"
    "2:\n\t"
    "s_mov_b32 m0, s42\n\t"
    "s_branch 9f\n\t"
    "8:\n\t"
    "s_mov_b32 s42, m0\n\t"
    "s_mov_b32 s41, 0\n\t"
    "s_mov_b32 s40, %[nt]\n\t"
    "s_mov_b32 s43, 0\n\t"
    "s_mov_b32 s44, 0x40000000\n\t"
    "s_mov_b32 s47, %[f0]\n\t"
    "v_mfma_f32_16x16x32_bf16 v[160:163], v[32:35], a[0:3], 0\n\t"
    "v_mfma_f32_16x16x32_bf16 v[164:167], v[32:35], a[4:7], 0\n\t"
    "v_mfma_f32_16x16x32_bf16 v[168:171], v[32:35], a[8:11], 0\n\t"
    "v_mfma_f32_16x16x32_bf16 v[172:175], v[32:35], a[12:15], 0\n\t"
    "v_mfma_f32_16x16x32_bf16 v[176:179], v[96:99], a[0:3], 0\n\t"
    "v_mfma_f32_16x16x32_bf16 v[180:183], v[96:99], a[4:7], 0\n\t"
    "v_mfma_f32_16x16x32_bf16 v[184:187], v[96:99], a[8:11], 0\n\t"
    "v_mfma_f32_16x16x32_bf16 v[188:191], v[96:99], a[12:15], 0\n\t"
    "v_mfma_f32_16x16x32_bf16 v[160:163], v[36:39], a[16:19], v[160:163]\n\t"
    "v_mfma_f32_16x16x32_bf16 v[164:167], v[36:39], a[20:23], v[164:167]\n\t"
    "v_mfma_f32_16x16x32_bf16 v[168:171], v[36:39], a[24:27], v[168:171]\n\t"
    "v_mfma_f32_16x16x32_bf16 v[172:175], v[36:39], a[28:31], v[172:175]\n\t"
    "v_mfma_f32_16x16x32_bf16 v[176:179], v[100:103], a[16:19], v[176:179]\n\t"
    "v_mfma_f32_16x16x32_bf16 v[180:183], v[100:103], a[20:23], v[180:183]\n\t"
    "v_mfma_f32_16x16x32_bf16 v[184:187], v[100:103], a[24:27], v[184:187]\n\t"
    "v_mfma_f32_16x16x32_bf16 v[188:191], v[100:103], a[28:31], v[188:191]\n\t"
    "v_mfma_f32_16x16x32_bf16 v[160:163], v[40:43], a[32:35], v[160:163]\n\t"
    "v_mfma_f32_16x16x32_bf16 v[164:167], v[40:43], a[36:39], v[164:167]\n\t"
    "v_mfma_f32_16x16x32_bf16 v[168:171], v[40:43], a[40:43], v[168:171]\n\t"
    "v_mfma_f32_16x16x32_bf16 v[172:175], v[40:43], a[44:47], v[172:175]\n\t"
    "v_mfma_f32_16x16x32_bf16 v[176:179], v[104:107], a[32:35], v[176:179]\n\t"
    "v_mfma_f32_16x16x32_bf16 v[180:183], v[104:107], a[36:39], v[180:183]\n\t"
    "v_mfma_f32_16x16x32_bf16 v[184:187], v[104:107], a[40:43], v[184:187]\n\t"
    "v_mfma_f32_16x16x32_bf16 v[188:191], v[104:107], a[44:47], v[188:191]\n\t"
    "v_mfma_f32_16x16x32_bf16 v[160:163], v[44:47], a[48:51], v[160:163]\n\t"
    "v_mfma_f32_16x16x32_bf16 v[164:167], v[44:47], a[52:55], v[164:167]\n\t"
    "v_mfma_f32_16x16x32_bf16 v[168:171], v[44:47], a[56:59], v[168:171]\n\t"
    "v_mfma_f32_16x16x32_bf16 v[172:175], v[44:47], a[60:63], v[172:175]\n\t"
    "v_mfma_f32_16x16x32_bf16 v[176:179], v[108:111], a[48:51], v[176:179]\n\t"
    "v_mfma_f32_16x16x32_bf16 v[180:183], v[108:111], a[52:55], v[180:183]\n\t"
    "v_mfma_f32_16x16x32_bf16 v[184:187], v[108:111], a[56:59], v[184:187]\n\t"
    "v_mfma_f32_16x16x32_bf16 v[188:191], v[108:111], a[60:63], v[188:191]\n\t"
    "v_mfma_f32_16x16x32_bf16 v[160:163], v[48:51], a[64:67], v[160:163]\n\t"
    "v_mfma_f32_16x16x32_bf16 v[164:167], v[48:51], a[68:71], v[164:167]\n\t"
    "v_mfma_f32_16x16x32_bf16 v[168:171], v[48:51], a[72:75], v[168:171]\n\t"
    "v_mfma_f32_16x16x32_bf16 v[172:175], v[48:51], a[76:79], v[172:175]\n\t"
    "v_mfma_f32_16x16x32_bf16 v[176:179], v[112:115], a[64:67], v[176:179]\n\t"
    "v_mfma_f32_16x16x32_bf16 v[180:183], v[112:115], a[68:71], v[180:183]\n\t"
    "v_mfma_f32_16x16x32_bf16 v[184:187], v[112:115], a[72:75], v[184:187]\n\t"
    "v_mfma_f32_16x16x32_bf16 v[188:191], v[112:115], a[76:79], v[188:191]\n\t"
    "v_mfma_f32_16x16x32_bf16 v[160:163], v[52:55], a[80:83], v[160:163]\n\t"
    "v_mfma_f32_16x16x32_bf16 v[164:167], v[52:55], a[84:87], v[164:167]\n\t"
    "v_mfma_f32_16x16x32_bf16 v[168:171], v[52:55], a[88:91], v[168:171]\n\t"
    "v_mfma_f32_16x16x32_bf16 v[172:175], v[52:55], a[92:95], v[172:175]\n\t"
    "v_mfma_f32_16x16x32_bf16 v[176:179], v[116:119], a[80:83], v[176:179]\n\t"
    "v_mfma_f32_16x16x32_bf16 v[180:183], v[116:119], a[84:87], v[180:183]\n\t"
    "v_mfma_f32_16x16x32_bf16 v[184:187], v[116:119], a[88:91], v[184:187]\n\t"
    "v_mfma_f32_16x16x32_bf16 v[188:191], v[116:119], a[92:95], v[188:191]\n\t"
    "v_mfma_f32_16x16x32_bf16 v[160:163], v[56:59], a[96:99], v[160:163]\n\t"
    "v_mfma_f32_16x16x32_bf16 v[164:167], v[56:59], a[100:103], v[164:167]\n\t"
    "v_mfma_f32_16x16x32_bf16 v[168:171], v[56:59], a[104:107], v[168:171]\n\t"
    "v_mfma_f32_16x16x32_bf16 v[172:175], v[56:59], a[108:111], v[172:175]\n\t"
    "v_mfma_f32_16x16x32_bf16 v[176:179], v[120:123], a[96:99], v[176:179]\n\t"
    "v_mfma_f32_16x16x32_bf16 v[180:183], v[120:123], a[100:103], v[180:183]\n\t"
    "v_mfma_f32_16x16x32_bf16 v[184:187], v[120:123], a[104:107], v[184:187]\n\t"
    "v_mfma_f32_16x16x32_bf16 v[188:191], v[120:123], a[108:111], v[188:191]\n\t"
    "v_mfma_f32_16x16x32_bf16 v[160:163], v[60:63], a[112:115], v[160:163]\n\t"
    "v_mfma_f32_16x16x32_bf16 v[164:167], v[60:63], a[116:119], v[164:167]\n\t"
    "v_mfma_f32_16x16x32_bf16 v[168:171], v[60:63], a[120:123], v[168:171]\n\t"
    "v_mfma_f32_16x16x32_bf16 v[172:175], v[60:63], a[124:127], v[172:175]\n\t"
    "v_mfma_f32_16x16x32_bf16 v[176:179], v[124:127], a[112:115], v[176:179]\n\t"
    "v_mfma_f32_16x16x32_bf16 v[180:183], v[124:127], a[116:119], v[180:183]\n\t"
    "v_mfma_f32_16x16x32_bf16 v[184:187], v[124:127], a[120:123], v[184:187]\n\t"
    "v_mfma_f32_16x16x32_bf16 v[188:191], v[124:127], a[124:127], v[188:191]\n\t"
    "v_mfma_f32_16x16x32_bf16 v[160:163], v[64:67], a[128:131], v[160:163]\n\t"
    "v_mfma_f32_16x16x32_bf16 v[164:167], v[64:67], a[132:135], v[164:167]\n\t"
    "v_mfma_f32_16x16x32_bf16 v[168:171], v[64:67], a[136:139], v[168:171]\n\t"
    "v_mfma_f32_16x16x32_bf16 v[172:175], v[64:67], a[140:143], v[172:175]\n\t"
    "v_mfma_f32_16x16x32_bf16 v[176:179], v[128:131], a[128:131], v[176:179]\n\t"
    "v_mfma_f32_16x16x32_bf16 v[180:183], v[128:131], a[132:135], v[180:183]\n\t"
    "v_mfma_f32_16x16x32_bf16 v[184:187], v[128:131], a[136:139], v[184:187]\n\t"
    "v_mfma_f32_16x16x32_bf16 v[188:191], v[128:131], a[140:143], v[188:191]\n\t"
    "v_mfma_f32_16x16x32_bf16 v[160:163], v[68:71], a[144:147], v[160:163]\n\t"
    "v_mfma_f32_16x16x32_bf16 v[164:167], v[68:71], a[148:151], v[164:167]\n\t"
    "v_mfma_f32_16x16x32_bf16 v[168:171], v[68:71], a[152:155], v[168:171]\n\t"
    "v_mfma_f32_16x16x32_bf16 v[172:175], v[68:71], a[156:159], v[172:175]\n\t"
    "v_mfma_f32_16x16x32_bf16 v[176:179], v[132:135], a[144:147], v[176:179]\n\t"
    "v_mfma_f32_16x16x32_bf16 v[180:183], v[132:135], a[148:151], v[180:183]\n\t"
    "v_mfma_f32_16x16x32_bf16 v[184:187], v[132:135], a[152:155], v[184:187]\n\t"
    "v_mfma_f32_16x16x32_bf16 v[188:191], v[132:135], a[156:159], v[188:191]\n\t"
    "v_mfma_f32_16x16x32_bf16 v[160:163], v[72:75], a[160:163], v[160:163]\n\t"
    "v_mfma_f32_16x16x32_bf16 v[164:167], v[72:75], a[164:167], v[164:167]\n\t"
    "v_mfma_f32_16x16x32_bf16 v[168:171], v[72:75], a[168:171], v[168:171]\n\t"
    "v_mfma_f32_16x16x32_bf16 v[172:175], v[72:75], a[172:175], v[172:175]\n\t"
    "v_mfma_f32_16x16x32_bf16 v[176:179], v[136:139], a[160:163], v[176:179]\n\t"
    "v_mfma_f32_16x16x32_bf16 v[180:183], v[136:139], a[164:167], v[180:183]\n\t"
    "v_mfma_f32_16x16x32_bf16 v[184:187], v[136:139], a[168:171], v[184:187]\n\t"
    "v_mfma_f32_16x16x32_bf16 v[188:191], v[136:139], a[172:175], v[188:191]\n\t"
    "v_mfma_f32_16x16x32_bf16 v[160:163], v[76:79], a[176:179], v[160:163]\n\t"
    "v_mfma_f32_16x16x32_bf16 v[164:167], v[76:79], a[180:183], v[164:167]\n\t"
    "v_mfma_f32_16x16x32_bf16 v[168:171], v[76:79], a[184:187], v[168:171]\n\t"
    "v_mfma_f32_16x16x32_bf16 v[172:175], v[76:79], a[188:191], v[172:175]\n\t"
    "v_mfma_f32_16x16x32_bf16 v[176:179], v[140:143], a[176:179], v[176:179]\n\t"
    "v_mfma_f32_16x16x32_bf16 v[180:183], v[140:143], a[180:183], v[180:183]\n\t"
    "v_mfma_f32_16x16x32_bf16 v[184:187], v[140:143], a[184:187], v[184:187]\n\t"
    "v_mfma_f32_16x16x32_bf16 v[188:191], v[140:143], a[188:191], v[188:191]\n\t"
    "v_mfma_f32_16x16x32_bf16 v[160:163], v[80:83], a[192:195], v[160:163]\n\t"
    "v_mfma_f32_16x16x32_bf16 v[164:167], v[80:83], a[196:199], v[164:167]\n\t"
    "v_mfma_f32_16x16x32_bf16 v[168:171], v[80:83], a[200:203], v[168:171]\n\t"
    "v_mfma_f32_16x16x32_bf16 v[172:175], v[80:83], a[204:207], v[172:175]\n\t"
    "v_mfma_f32_16x16x32_bf16 v[176:179], v[144:147], a[192:195], v[176:179]\n\t"
    "v_mfma_f32_16x16x32_bf16 v[180:183], v[144:147], a[196:199], v[180:183]\n\t"
    "v_mfma_f32_16x16x32_bf16 v[184:187], v[144:147], a[200:203], v[184:187]\n\t"
    "v_mfma_f32_16x16x32_bf16 v[188:191], v[144:147], a[204:207], v[188:191]\n\t"
    "v_mfma_f32_16x16x32_bf16 v[160:163], v[84:87], a[208:211], v[160:163]\n\t"
    "v_mfma_f32_16x16x32_bf16 v[164:167], v[84:87], a[212:215], v[164:167]\n\t"
    "v_mfma_f32_16x16x32_bf16 v[168:171], v[84:87], a[216:219], v[168:171]\n\t"
    "v_mfma_f32_16x16x32_bf16 v[172:175], v[84:87], a[220:223], v[172:175]\n\t"
    "v_mfma_f32_16x16x32_bf16 v[176:179], v[148:151], a[208:211], v[176:179]\n\t"
    "v_mfma_f32_16x16x32_bf16 v[180:183], v[148:151], a[212:215], v[180:183]\n\t"
    "v_mfma_f32_16x16x32_bf16 v[184:187], v[148:151], a[216:219], v[184:187]\n\t"
    "v_mfma_f32_16x16x32_bf16 v[188:191], v[148:151], a[220:223], v[188:191]\n\t"
    "v_mfma_f32_16x16x32_bf16 v[160:163], v[88:91], a[224:227], v[160:163]\n\t"
    "v_mfma_f32_16x16x32_bf16 v[164:167], v[88:91], a[228:231], v[164:167]\n\t"
    "v_mfma_f32_16x16x32_bf16 v[168:171], v[88:91], a[232:235], v[168:171]\n\t"
    "v_mfma_f32_16x16x32_bf16 v[172:175], v[88:91], a[236:239], v[172:175]\n\t"
    "v_mfma_f32_16x16x32_bf16 v[176:179], v[152:155], a[224:227], v[176:179]\n\t"
    "v_mfma_f32_16x16x32_bf16 v[180:183], v[152:155], a[228:231], v[180:183]\n\t"
    "v_mfma_f32_16x16x32_bf16 v[184:187], v[152:155], a[232:235], v[184:187]\n\t"
    "v_mfma_f32_16x16x32_bf16 v[188:191], v[152:155], a[236:239], v[188:191]\n\t"
    "v_mfma_f32_16x16x32_bf16 v[160:163], v[92:95], a[240:243], v[160:163]\n\t"
    "v_mfma_f32_16x16x32_bf16 v[164:167], v[92:95], a[244:247], v[164:167]\n\t"
    "v_mfma_f32_16x16x32_bf16 v[168:171], v[92:95], a[248:251], v[168:171]\n\t"
    "v_mfma_f32_16x16x32_bf16 v[172:175], v[92:95], a[252:255], v[172:175]\n\t"
    "v_mfma_f32_16x16x32_bf16 v[176:179], v[156:159], a[240:243], v[176:179]\n\t"
    "v_mfma_f32_16x16x32_bf16 v[180:183], v[156:159], a[244:247], v[180:183]\n\t"
    "v_mfma_f32_16x16x32_bf16 v[184:187], v[156:159], a[248:251], v[184:187]\n\t"
    "v_mfma_f32_16x16x32_bf16 v[188:191], v[156:159], a[252:255], v[188:191]\n\t"
    "s_nop 0\n\t"
    "v_cvt_pk_bf16_f32 v192, v160, v161\n\t"
    "v_cvt_pk_bf16_f32 v193, v162, v163\n\t"
    "s_nop 1\n\t"
    "v_cvt_pk_bf16_f32 v194, v176, v177\n\t"
    "v_cvt_pk_bf16_f32 v195, v178, v179\n\t"
    "buffer_store_dwordx4 v[192:195], v10, %[rd], 0 offen offset:0 sc1\n\t"
    "v_cvt_pk_bf16_f32 v196, v164, v165\n\t"
    "v_cvt_pk_bf16_f32 v197, v166, v167\n\t"
    "v_cvt_pk_bf16_f32 v198, v180, v181\n\t"
    "v_cvt_pk_bf16_f32 v199, v182, v183\n\t"
    "buffer_store_dwordx4 v[196:199], v10, %[rd], 0 offen offset:1024 sc1\n\t"
    "v_cvt_pk_bf16_f32 v200, v168, v169\n\t"
    "v_cvt_pk_bf16_f32 v201, v170, v171\n\t"
    "v_cvt_pk_bf16_f32 v202, v184, v185\n\t"
    "v_cvt_pk_bf16_f32 v203, v186, v187\n\t"
    "buffer_store_dwordx4 v[200:203], v10, %[rd], 0 offen offset:2048 sc1\n\t"
    "v_cvt_pk_bf16_f32 v204, v172, v173\n\t"
    "v_cvt_pk_bf16_f32 v205, v174, v175\n\t"
    "v_cvt_pk_bf16_f32 v206, v188, v189\n\t"
    "v_cvt_pk_bf16_f32 v207, v190, v191\n\t"
    "buffer_store_dwordx4 v[204:207], v10, %[rd], 0 offen offset:3072 sc1\n\t"
    "s_add_u32 s46, s41, 0\n\t"
    "s_add_u32 m0, %[mb], 0\n\t"
    "s_nop 0\n\t"
    "buffer_load_dwordx4 v0, %[rx], s46 offen lds\n\t"
    "s_add_u32 m0, %[mb], 1024\n\t"
    "s_nop 0\n\t"
    "buffer_load_dwordx4 v1, %[rx], s46 offen lds\n\t"
    "s_add_u32 m0, %[mb], 2048\n\t"
    "s_nop 0\n\t"
    "buffer_load_dwordx4 v2, %[rx], s46 offen lds\n\t"
    "s_add_u32 m0, %[mb], 3072\n\t"
    "s_nop 0\n\t"
    "buffer_load_dwordx4 v3, %[rx], s46 offen lds\n\t"
    "s_add_u32 s46, s41, 128\n\t"
    "s_add_u32 m0, %[mb], 16384\n\t"
    "s_nop 0\n\t"
    "buffer_load_dwordx4 v0, %[rx], s46 offen lds\n\t"
    "s_add_u32 m0, %[mb], 17408\n\t"
    "s_nop 0\n\t"
    "buffer_load_dwordx4 v1, %[rx], s46 offen lds\n\t"
    "s_add_u32 m0, %[mb], 18432\n\t"
    "s_nop 0\n\t"
    "buffer_load_dwordx4 v2, %[rx], s46 offen lds\n\t"
    "s_add_u32 m0, %[mb], 19456\n\t"
    "s_nop 0\n\t"
    "buffer_load_dwordx4 v3, %[rx], s46 offen lds\n\t"
    "s_waitcnt vmcnt(8)\n\t"
    "s_barrier\n\t"
    "s_cmp_lg_u32 %[w], 0\n\t"
    "s_cbranch_scc1 2f\n\t"
    "s_mov_b64 exec, 1\n\t"
    "buffer_store_dword v9, off, %[rc], %[fm] sc1\n\t"
    "s_mov_b64 exec, -1\n\t"
    "2:\n\t"
    "s_add_u32 s46, s41, 256\n\t"
    "s_add_u32 m0, %[mb], 32768\n\t"
    "s_nop 0\n\t"
    "buffer_load_dwordx4 v0, %[rx], s46 offen lds\n\t"
    "s_add_u32 m0, %[mb], 33792\n\t"
    "s_nop 0\n\t"
    "buffer_load_dwordx4 v1, %[rx], s46 offen lds\n\t"
    "s_add_u32 m0, %[mb], 34816\n\t"
    "s_nop 0\n\t"
    "buffer_load_dwordx4 v2, %[rx], s46 offen lds\n\t"
    "s_add_u32 m0, %[mb], 35840\n\t"
    "s_nop 0\n\t"
    "buffer_load_dwordx4 v3, %[rx], s46 offen lds\n\t"
    "s_add_u32 s46, s41, 384\n\t"
    "s_add_u32 m0, %[mb], 49152\n\t"
    "s_nop 0\n\t"
    "buffer_load_dwordx4 v0, %[rx], s46 offen lds\n\t"
    "s_add_u32 m0, %[mb], 50176\n\t"
    "s_nop 0\n\t"
    "buffer_load_dwordx4 v1, %[rx], s46 offen lds\n\t"
    "s_add_u32 m0, %[mb], 51200\n\t"
    "s_nop 0\n\t"
    "buffer_load_dwordx4 v2, %[rx], s46 offen lds\n\t"
    "s_add_u32 m0, %[mb], 52224\n\t"
    "s_nop 0\n\t"
    "buffer_load_dwordx4 v3, %[rx], s46 offen lds\n\t"
    "s_add_u32 s46, s41, 512\n\t"
    "s_add_u32 m0, %[mb], 65536\n\t"
    "s_nop 0\n\t"
    "buffer_load_dwordx4 v0, %[rx], s46 offen lds\n\t"
    "s_add_u32 m0, %[mb], 66560\n\t"
    "s_nop 0\n\t"
    "buffer_load_dwordx4 v1, %[rx], s46 offen lds\n\t"
    "s_add_u32 m0, %[mb], 67584\n\t"
    "s_nop 0\n\t"
    "buffer_load_dwordx4 v2, %[rx], s46 offen lds\n\t"
    "s_add_u32 m0, %[mb], 68608\n\t"
    "s_nop 0\n\t"
    "buffer_load_dwordx4 v3, %[rx], s46 offen lds\n\t"
    "s_add_u32 s46, s41, 640\n\t"
    "s_add_u32 m0, %[mb], 81920\n\t"
    "s_nop 0\n\t"
    "buffer_load_dwordx4 v0, %[rx], s46 offen lds\n\t"
    "s_add_u32 m0, %[mb], 82944\n\t"
    "s_nop 0\n\t"
    "buffer_load_dwordx4 v1, %[rx], s46 offen lds\n\t"
    "s_add_u32 m0, %[mb], 83968\n\t"
    "s_nop 0\n\t"
    "buffer_load_dwordx4 v2, %[rx], s46 offen lds\n\t"
    "s_add_u32 m0, %[mb], 84992\n\t"
    "s_nop 0\n\t"
    "buffer_load_dwordx4 v3, %[rx], s46 offen lds\n\t"
    "s_add_u32 s46, s41, 768\n\t"
    "s_add_u32 m0, %[mb], 98304\n\t"
    "s_nop 0\n\t"
    "buffer_load_dwordx4 v0, %[rx], s46 offen lds\n\t"
    "s_add_u32 m0, %[mb], 99328\n\t"
    "s_nop 0\n\t"
    "buffer_load_dwordx4 v1, %[rx], s46 offen lds\n\t"
    "s_add_u32 m0, %[mb], 100352\n\t"
    "s_nop 0\n\t"
    "buffer_load_dwordx4 v2, %[rx], s46 offen lds\n\t"
    "s_add_u32 m0, %[mb], 101376\n\t"
    "s_nop 0\n\t"
    "buffer_load_dwordx4 v3, %[rx], s46 offen lds\n\t"
    "s_add_u32 s46, s41, 896\n\t"
    "s_add_u32 m0, %[mb], 114688\n\t"
    "s_nop 0\n\t"
    "buffer_load_dwordx4 v0, %[rx], s46 offen lds\n\t"
    "s_add_u32 m0, %[mb], 115712\n\t"
    "s_nop 0\n\t"
    "buffer_load_dwordx4 v1, %[rx], s46 offen lds\n\t"
    "s_add_u32 m0, %[mb], 116736\n\t"
    "s_nop 0\n\t"
    "buffer_load_dwordx4 v2, %[rx], s46 offen lds\n\t"
    "s_add_u32 m0, %[mb], 117760\n\t"
    "s_nop 0\n\t"
    "buffer_load_dwordx4 v3, %[rx], s46 offen lds\n\t"
    "s_mov_b32 s46, %[sg]\n\t"
    "4:\n\t"
    "s_cmp_eq_u32 s46, 0\n\t"
    "s_cbranch_scc1 5f\n\t"
    "s_sleep 6\n\t"
    "s_sub_u32 s46, s46, 1\n\t"
    "s_branch 4b\n\t"
    "5:\n\t"
    "s_waitcnt vmcnt(28)\n\t"
    "s_barrier\n\t"
    "ds_read_b128 v[32:35], v4 offset:0\n\t"
    "ds_read_b128 v[36:39], v4 offset:2048\n\t"
    "ds_read_b128 v[40:43], v4 offset:4096\n\t"
    "ds_read_b128 v[44:47], v4 offset:6144\n\t"
    "ds_read_b128 v[48:51], v4 offset:8192\n\t"
    "ds_read_b128 v[52:55], v4 offset:10240\n\t"
    "ds_read_b128 v[56:59], v4 offset:12288\n\t"
    "ds_read_b128 v[60:63], v4 offset:14336\n\t"
    "1:\n\t"
    "s_add_u32 s41, s41, %[xt]\n\t"
    "s_cmp_eq_u32 s40, 1\n\t"
    "s_cselect_b32 s41, 0x40000000, s41\n\t"
    "s_waitcnt lgkmcnt(7)\n\t"
    "v_mfma_f32_16x16x32_bf16 v[128:131], v[32:35], a[0:3], 0\n\t"
    "ds_read_b128 v[64:67], v6 offset:0\n\t"
    "v_mfma_f32_16x16x32_bf16 v[132:135], v[32:35], a[4:7], 0\n\t"
    "v_mfma_f32_16x16x32_bf16 v[136:139], v[32:35], a[8:11], 0\n\t"
    "ds_read_b128 v[68:71], v6 offset:2048\n\t"
    "v_mfma_f32_16x16x32_bf16 v[140:143], v[32:35], a[12:15], 0\n\t"
    "s_waitcnt lgkmcnt(8)\n\t"
    "v_mfma_f32_16x16x32_bf16 v[144:147], v[36:39], a[0:3], 0\n\t"
    "ds_read_b128 v[72:75], v6 offset:4096\n\t"
    "v_mfma_f32_16x16x32_bf16 v[148:151], v[36:39], a[4:7], 0\n\t"
    "v_mfma_f32_16x16x32_bf16 v[152:155], v[36:39], a[8:11], 0\n\t"
    "ds_read_b128 v[76:79], v6 offset:6144\n\t"
    "v_mfma_f32_16x16x32_bf16 v[156:159], v[36:39], a[12:15], 0\n\t"
    "s_waitcnt lgkmcnt(9)\n\t"
    "v_mfma_f32_16x16x32_bf16 v[160:163], v[40:43], a[0:3], 0\n\t"
    "ds_read_b128 v[80:83], v6 offset:8192\n\t"
    "v_mfma_f32_16x16x32_bf16 v[164:167], v[40:43], a[4:7], 0\n\t"
    "v_mfma_f32_16x16x32_bf16 v[168:171], v[40:43], a[8:11], 0\n\t"
    "ds_read_b128 v[84:87], v6 offset:10240\n\t"
    "v_mfma_f32_16x16x32_bf16 v[172:175], v[40:43], a[12:15], 0\n\t"
    "s_waitcnt lgkmcnt(10)\n\t"
    "v_mfma_f32_16x16x32_bf16 v[176:179], v[44:47], a[0:3], 0\n\t"
    "ds_read_b128 v[88:91], v6 offset:12288\n\t"
    "v_mfma_f32_16x16x32_bf16 v[180:183], v[44:47], a[4:7], 0\n\t"
    "v_mfma_f32_16x16x32_bf16 v[184:187], v[44:47], a[8:11], 0\n\t"
    "ds_read_b128 v[92:95], v6 offset:14336\n\t"
    "v_mfma_f32_16x16x32_bf16 v[188:191], v[44:47], a[12:15], 0\n\t"
    "s_waitcnt lgkmcnt(11)\n\t"
    "v_mfma_f32_16x16x32_bf16 v[192:195], v[48:51], a[0:3], 0\n\t"
    "v_mfma_f32_16x16x32_bf16 v[196:199], v[48:51], a[4:7], 0\n\t"
    "v_mfma_f32_16x16x32_bf16 v[200:203], v[48:51], a[8:11], 0\n\t"
    "v_mfma_f32_16x16x32_bf16 v[204:207], v[48:51], a[12:15], 0\n\t"
    "s_waitcnt lgkmcnt(10)\n\t"
    "v_mfma_f32_16x16x32_bf16 v[208:211], v[52:55], a[0:3], 0\n\t"
    "v_mfma_f32_16x16x32_bf16 v[212:215], v[52:55], a[4:7], 0\n\t"
    "v_mfma_f32_16x16x32_bf16 v[216:219], v[52:55], a[8:11], 0\n\t"
    "v_mfma_f32_16x16x32_bf16 v[220:223], v[52:55], a[12:15], 0\n\t"
    "s_waitcnt lgkmcnt(9)\n\t"
    "v_mfma_f32_16x16x32_bf16 v[224:227], v[56:59], a[0:3], 0\n\t"
    "v_mfma_f32_16x16x32_bf16 v[228:231], v[56:59], a[4:7], 0\n\t"
    "v_mfma_f32_16x16x32_bf16 v[232:235], v[56:59], a[8:11], 0\n\t"
    "v_mfma_f32_16x16x32_bf16 v[236:239], v[56:59], a[12:15], 0\n\t"
    "s_waitcnt lgkmcnt(8)\n\t"
    "v_mfma_f32_16x16x32_bf16 v[240:243], v[60:63], a[0:3], 0\n\t"
    "v_mfma_f32_16x16x32_bf16 v[244:247], v[60:63], a[4:7], 0\n\t"
    "v_mfma_f32_16x16x32_bf16 v[248:251], v[60:63], a[8:11], 0\n\t"
    "v_mfma_f32_16x16x32_bf16 v[252:255], v[60:63], a[12:15], 0\n\t"
    "s_waitcnt vmcnt(0)\n\t"
    "s_waitcnt lgkmcnt(0)\n\t"
    "s_barrier\n\t"
    "s_cmp_lg_u32 %[w], 0\n\t"
    "s_cbranch_scc1 2f\n\t"
    "s_mov_b64 exec, 1\n\t"
    "buffer_store_dword v9, off, %[rc], s44 sc1\n\t"
    "s_mov_b64 exec, -1\n\t"
    "2:\n\t"
    "v_mfma_f32_16x16x32_bf16 v[128:131], v[64:67], a[16:19], v[128:131]\n\t"
    "ds_read_b128 v[32:35], v4 offset:16384\n\t"
    "v_mfma_f32_16x16x32_bf16 v[132:135], v[64:67], a[20:23], v[132:135]\n\t"
    "v_mfma_f32_16x16x32_bf16 v[136:139], v[64:67], a[24:27], v[136:139]\n\t"
    "ds_read_b128 v[36:39], v4 offset:18432\n\t"
    "v_mfma_f32_16x16x32_bf16 v[140:143], v[64:67], a[28:31], v[140:143]\n\t"
    "s_add_u32 s46, s41, 0\n\t"
    "s_add_u32 m0, %[mb], 0\n\t"
    "s_nop 0\n\t"
    "buffer_load_dwordx4 v0, %[rx], s46 offen lds\n\t"
    "v_mfma_f32_16x16x32_bf16 v[144:147], v[68:71], a[16:19], v[144:147]\n\t"
    "ds_read_b128 v[40:43], v4 offset:20480\n\t"
    "v_mfma_f32_16x16x32_bf16 v[148:151], v[68:71], a[20:23], v[148:151]\n\t"
    "v_mfma_f32_16x16x32_bf16 v[152:155], v[68:71], a[24:27], v[152:155]\n\t"
    "ds_read_b128 v[44:47], v4 offset:22528\n\t"
    "v_mfma_f32_16x16x32_bf16 v[156:159], v[68:71], a[28:31], v[156:159]\n\t"
    "v_mfma_f32_16x16x32_bf16 v[160:163], v[72:75], a[16:19], v[160:163]\n\t"
    "ds_read_b128 v[48:51], v4 offset:24576\n\t"
    "v_mfma_f32_16x16x32_bf16 v[164:167], v[72:75], a[20:23], v[164:167]\n\t"
    "s_add_u32 m0, %[mb], 1024\n\t"
    "s_nop 0\n\t"
    "buffer_load_dwordx4 v1, %[rx], s46 offen lds\n\t"
    "v_mfma_f32_16x16x32_bf16 v[168:171], v[72:75], a[24:27], v[168:171]\n\t"
    "ds_read_b128 v[52:55], v4 offset:26624\n\t"
    "v_mfma_f32_16x16x32_bf16 v[172:175], v[72:75], a[28:31], v[172:175]\n\t"
    "v_mfma_f32_16x16x32_bf16 v[176:179], v[76:79], a[16:19], v[176:179]\n\t"
    "ds_read_b128 v[56:59], v4 offset:28672\n\t"
    "v_mfma_f32_16x16x32_bf16 v[180:183], v[76:79], a[20:23], v[180:183]\n\t"
    "v_mfma_f32_16x16x32_bf16 v[184:187], v[76:79], a[24:27], v[184:187]\n\t"
    "ds_read_b128 v[60:63], v4 offset:30720\n\t"
    "v_mfma_f32_16x16x32_bf16 v[188:191], v[76:79], a[28:31], v[188:191]\n\t"
    "s_add_u32 m0, %[mb], 2048\n\t"
    "s_nop 0\n\t"
    "buffer_load_dwordx4 v2, %[rx], s46 offen lds\n\t"
    "v_mfma_f32_16x16x32_bf16 v[192:195], v[80:83], a[16:19], v[192:195]\n\t"
    "v_mfma_f32_16x16x32_bf16 v[196:199], v[80:83], a[20:23], v[196:199]\n\t"
    "v_mfma_f32_16x16x32_bf16 v[200:203], v[80:83], a[24:27], v[200:203]\n\t"
    "v_mfma_f32_16x16x32_bf16 v[204:207], v[80:83], a[28:31], v[204:207]\n\t"
    "v_mfma_f32_16x16x32_bf16 v[208:211], v[84:87], a[16:19], v[208:211]\n\t"
    "v_mfma_f32_16x16x32_bf16 v[212:215], v[84:87], a[20:23], v[212:215]\n\t"
    "s_add_u32 m0, %[mb], 3072\n\t"
    "s_nop 0\n\t"
    "buffer_load_dwordx4 v3, %[rx], s46 offen lds\n\t"
    "v_mfma_f32_16x16x32_bf16 v[216:219], v[84:87], a[24:27], v[216:219]\n\t"
    "v_mfma_f32_16x16x32_bf16 v[220:223], v[84:87], a[28:31], v[220:223]\n\t"
    "v_mfma_f32_16x16x32_bf16 v[224:227], v[88:91], a[16:19], v[224:227]\n\t"
    "v_mfma_f32_16x16x32_bf16 v[228:231], v[88:91], a[20:23], v[228:231]\n\t"
    "v_mfma_f32_16x16x32_bf16 v[232:235], v[88:91], a[24:27], v[232:235]\n\t"
    "v_mfma_f32_16x16x32_bf16 v[236:239], v[88:91], a[28:31], v[236:239]\n\t"
    "v_mfma_f32_16x16x32_bf16 v[240:243], v[92:95], a[16:19], v[240:243]\n\t"
    "v_mfma_f32_16x16x32_bf16 v[244:247], v[92:95], a[20:23], v[244:247]\n\t"
    "v_mfma_f32_16x16x32_bf16 v[248:251], v[92:95], a[24:27], v[248:251]\n\t"
    "v_mfma_f32_16x16x32_bf16 v[252:255], v[92:95], a[28:31], v[252:255]\n\t"
    "s_waitcnt lgkmcnt(7)\n\t"
    "v_mfma_f32_16x16x32_bf16 v[128:131], v[32:35], a[32:35], v[128:131]\n\t"
    "ds_read_b128 v[64:67], v6 offset:16384\n\t"
    "v_mfma_f32_16x16x32_bf16 v[132:135], v[32:35], a[36:39], v[132:135]\n\t"
    "v_mfma_f32_16x16x32_bf16 v[136:139], v[32:35], a[40:43], v[136:139]\n\t"
    "ds_read_b128 v[68:71], v6 offset:18432\n\t"
    "v_mfma_f32_16x16x32_bf16 v[140:143], v[32:35], a[44:47], v[140:143]\n\t"
    "s_waitcnt lgkmcnt(8)\n\t"
    "v_mfma_f32_16x16x32_bf16 v[144:147], v[36:39], a[32:35], v[144:147]\n\t"
    "ds_read_b128 v[72:75], v6 offset:20480\n\t"
    "v_mfma_f32_16x16x32_bf16 v[148:151], v[36:39], a[36:39], v[148:151]\n\t"
    "v_mfma_f32_16x16x32_bf16 v[152:155], v[36:39], a[40:43], v[152:155]\n\t"
    "ds_read_b128 v[76:79], v6 offset:22528\n\t"
    "v_mfma_f32_16x16x32_bf16 v[156:159], v[36:39], a[44:47], v[156:159]\n\t"
    "s_waitcnt lgkmcnt(9)\n\t"
    "v_mfma_f32_16x16x32_bf16 v[160:163], v[40:43], a[32:35], v[160:163]\n\t"
    "ds_read_b128 v[80:83], v6 offset:24576\n\t"
    "v_mfma_f32_16x16x32_bf16 v[164:167], v[40:43], a[36:39], v[164:167]\n\t"
    "v_mfma_f32_16x16x32_bf16 v[168:171], v[40:43], a[40:43], v[168:171]\n\t"
    "ds_read_b128 v[84:87], v6 offset:26624\n\t"
    "v_mfma_f32_16x16x32_bf16 v[172:175], v[40:43], a[44:47], v[172:175]\n\t"
    "s_waitcnt lgkmcnt(10)\n\t"
    "v_mfma_f32_16x16x32_bf16 v[176:179], v[44:47], a[32:35], v[176:179]\n\t"
    "ds_read_b128 v[88:91], v6 offset:28672\n\t"
    "v_mfma_f32_16x16x32_bf16 v[180:183], v[44:47], a[36:39], v[180:183]\n\t"
    "v_mfma_f32_16x16x32_bf16 v[184:187], v[44:47], a[40:43], v[184:187]\n\t"
    "ds_read_b128 v[92:95], v6 offset:30720\n\t"
    "v_mfma_f32_16x16x32_bf16 v[188:191], v[44:47], a[44:47], v[188:191]\n\t"
    "s_waitcnt lgkmcnt(11)\n\t"
    "v_mfma_f32_16x16x32_bf16 v[192:195], v[48:51], a[32:35], v[192:195]\n\t"
    "v_mfma_f32_16x16x32_bf16 v[196:199], v[48:51], a[36:39], v[196:199]\n\t"
    "v_mfma_f32_16x16x32_bf16 v[200:203], v[48:51], a[40:43], v[200:203]\n\t"
    "v_mfma_f32_16x16x32_bf16 v[204:207], v[48:51], a[44:47], v[204:207]\n\t"
    "s_waitcnt lgkmcnt(10)\n\t"
    "v_mfma_f32_16x16x32_bf16 v[208:211], v[52:55], a[32:35], v[208:211]\n\t"
    "v_mfma_f32_16x16x32_bf16 v[212:215], v[52:55], a[36:39], v[212:215]\n\t"
    "v_mfma_f32_16x16x32_bf16 v[216:219], v[52:55], a[40:43], v[216:219]\n\t"
    "v_mfma_f32_16x16x32_bf16 v[220:223], v[52:55], a[44:47], v[220:223]\n\t"
    "s_waitcnt lgkmcnt(9)\n\t"
    "v_mfma_f32_16x16x32_bf16 v[224:227], v[56:59], a[32:35], v[224:227]\n\t"
    "v_mfma_f32_16x16x32_bf16 v[228:231], v[56:59], a[36:39], v[228:231]\n\t"
    "v_mfma_f32_16x16x32_bf16 v[232:235], v[56:59], a[40:43], v[232:235]\n\t"
    "v_mfma_f32_16x16x32_bf16 v[236:239], v[56:59], a[44:47], v[236:239]\n\t"
    "s_waitcnt lgkmcnt(8)\n\t"
    "v_mfma_f32_16x16x32_bf16 v[240:243], v[60:63], a[32:35], v[240:243]\n\t"
    "v_mfma_f32_16x16x32_bf16 v[244:247], v[60:63], a[36:39], v[244:247]\n\t"
    "v_mfma_f32_16x16x32_bf16 v[248:251], v[60:63], a[40:43], v[248:251]\n\t"
    "v_mfma_f32_16x16x32_bf16 v[252:255], v[60:63], a[44:47], v[252:255]\n\t"
    "s_waitcnt vmcnt(24)\n\t"
    "s_waitcnt lgkmcnt(0)\n\t"
    "s_barrier\n\t"
    "v_mfma_f32_16x16x32_bf16 v[128:131], v[64:67], a[48:51], v[128:131]\n\t"
    "ds_read_b128 v[32:35], v4 offset:32768\n\t"
    "v_mfma_f32_16x16x32_bf16 v[132:135], v[64:67], a[52:55], v[132:135]\n\t"
    "v_mfma_f32_16x16x32_bf16 v[136:139], v[64:67], a[56:59], v[136:139]\n\t"
    "ds_read_b128 v[36:39], v4 offset:34816\n\t"
    "v_mfma_f32_16x16x32_bf16 v[140:143], v[64:67], a[60:63], v[140:143]\n\t"
    "s_add_u32 s46, s41, 128\n\t"
    "s_add_u32 m0, %[mb], 16384\n\t"
    "s_nop 0\n\t"
    "buffer_load_dwordx4 v0, %[rx], s46 offen lds\n\t"
    "v_mfma_f32_16x16x32_bf16 v[144:147], v[68:71], a[48:51], v[144:147]\n\t"
    "ds_read_b128 v[40:43], v4 offset:36864\n\t"
    "v_mfma_f32_16x16x32_bf16 v[148:151], v[68:71], a[52:55], v[148:151]\n\t"
    "v_mfma_f32_16x16x32_bf16 v[152:155], v[68:71], a[56:59], v[152:155]\n\t"
    "ds_read_b128 v[44:47], v4 offset:38912\n\t"
    "v_mfma_f32_16x16x32_bf16 v[156:159], v[68:71], a[60:63], v[156:159]\n\t"
    "v_mfma_f32_16x16x32_bf16 v[160:163], v[72:75], a[48:51], v[160:163]\n\t"
    "ds_read_b128 v[48:51], v4 offset:40960\n\t"
    "v_mfma_f32_16x16x32_bf16 v[164:167], v[72:75], a[52:55], v[164:167]\n\t"
    "s_add_u32 m0, %[mb], 17408\n\t"
    "s_nop 0\n\t"
    "buffer_load_dwordx4 v1, %[rx], s46 offen lds\n\t"
    "v_mfma_f32_16x16x32_bf16 v[168:171], v[72:75], a[56:59], v[168:171]\n\t"
    "ds_read_b128 v[52:55], v4 offset:43008\n\t"
    "v_mfma_f32_16x16x32_bf16 v[172:175], v[72:75], a[60:63], v[172:175]\n\t"
    "v_mfma_f32_16x16x32_bf16 v[176:179], v[76:79], a[48:51], v[176:179]\n\t"
    "ds_read_b128 v[56:59], v4 offset:45056\n\t"
    "v_mfma_f32_16x16x32_bf16 v[180:183], v[76:79], a[52:55], v[180:183]\n\t"
    "v_mfma_f32_16x16x32_bf16 v[184:187], v[76:79], a[56:59], v[184:187]\n\t"
    "ds_read_b128 v[60:63], v4 offset:47104\n\t"
    "v_mfma_f32_16x16x32_bf16 v[188:191], v[76:79], a[60:63], v[188:191]\n\t"
    "s_add_u32 m0, %[mb], 18432\n\t"
    "s_nop 0\n\t"
    "buffer_load_dwordx4 v2, %[rx], s46 offen lds\n\t"
    "v_mfma_f32_16x16x32_bf16 v[192:195], v[80:83], a[48:51], v[192:195]\n\t"
    "v_mfma_f32_16x16x32_bf16 v[196:199], v[80:83], a[52:55], v[196:199]\n\t"
    "v_mfma_f32_16x16x32_bf16 v[200:203], v[80:83], a[56:59], v[200:203]\n\t"
    "v_mfma_f32_16x16x32_bf16 v[204:207], v[80:83], a[60:63], v[204:207]\n\t"
    "v_mfma_f32_16x16x32_bf16 v[208:211], v[84:87], a[48:51], v[208:211]\n\t"
    "v_mfma_f32_16x16x32_bf16 v[212:215], v[84:87], a[52:55], v[212:215]\n\t"
    "s_add_u32 m0, %[mb], 19456\n\t"
    "s_nop 0\n\t"
    "buffer_load_dwordx4 v3, %[rx], s46 offen lds\n\t"
    "v_mfma_f32_16x16x32_bf16 v[216:219], v[84:87], a[56:59], v[216:219]\n\t"
    "v_mfma_f32_16x16x32_bf16 v[220:223], v[84:87], a[60:63], v[220:223]\n\t"
    "v_mfma_f32_16x16x32_bf16 v[224:227], v[88:91], a[48:51], v[224:227]\n\t"
    "v_mfma_f32_16x16x32_bf16 v[228:231], v[88:91], a[52:55], v[228:231]\n\t"
    "v_mfma_f32_16x16x32_bf16 v[232:235], v[88:91], a[56:59], v[232:235]\n\t"
    "v_mfma_f32_16x16x32_bf16 v[236:239], v[88:91], a[60:63], v[236:239]\n\t"
    "v_mfma_f32_16x16x32_bf16 v[240:243], v[92:95], a[48:51], v[240:243]\n\t"
    "v_mfma_f32_16x16x32_bf16 v[244:247], v[92:95], a[52:55], v[244:247]\n\t"
    "v_mfma_f32_16x16x32_bf16 v[248:251], v[92:95], a[56:59], v[248:251]\n\t"
    "v_mfma_f32_16x16x32_bf16 v[252:255], v[92:95], a[60:63], v[252:255]\n\t"
    "s_waitcnt lgkmcnt(7)\n\t"
    "v_mfma_f32_16x16x32_bf16 v[128:131], v[32:35], a[64:67], v[128:131]\n\t"
    "ds_read_b128 v[64:67], v6 offset:32768\n\t"
    "v_mfma_f32_16x16x32_bf16 v[132:135], v[32:35], a[68:71], v[132:135]\n\t"
    "v_mfma_f32_16x16x32_bf16 v[136:139], v[32:35], a[72:75], v[136:139]\n\t"
    "ds_read_b128 v[68:71], v6 offset:34816\n\t"
    "v_mfma_f32_16x16x32_bf16 v[140:143], v[32:35], a[76:79], v[140:143]\n\t"
    "s_waitcnt lgkmcnt(8)\n\t"
    "v_mfma_f32_16x16x32_bf16 v[144:147], v[36:39], a[64:67], v[144:147]\n\t"
    "ds_read_b128 v[72:75], v6 offset:36864\n\t"
    "v_mfma_f32_16x16x32_bf16 v[148:151], v[36:39], a[68:71], v[148:151]\n\t"
    "v_mfma_f32_16x16x32_bf16 v[152:155], v[36:39], a[72:75], v[152:155]\n\t"
    "ds_read_b128 v[76:79], v6 offset:38912\n\t"
    "v_mfma_f32_16x16x32_bf16 v[156:159], v[36:39], a[76:79], v[156:159]\n\t"
    "s_waitcnt lgkmcnt(9)\n\t"
    "v_mfma_f32_16x16x32_bf16 v[160:163], v[40:43], a[64:67], v[160:163]\n\t"
    "ds_read_b128 v[80:83], v6 offset:40960\n\t"
    "v_mfma_f32_16x16x32_bf16 v[164:167], v[40:43], a[68:71], v[164:167]\n\t"
    "v_mfma_f32_16x16x32_bf16 v[168:171], v[40:43], a[72:75], v[168:171]\n\t"
    "ds_read_b128 v[84:87], v6 offset:43008\n\t"
    "v_mfma_f32_16x16x32_bf16 v[172:175], v[40:43], a[76:79], v[172:175]\n\t"
    "s_waitcnt lgkmcnt(10)\n\t"
    "v_mfma_f32_16x16x32_bf16 v[176:179], v[44:47], a[64:67], v[176:179]\n\t"
    "ds_read_b128 v[88:91], v6 offset:45056\n\t"
    "v_mfma_f32_16x16x32_bf16 v[180:183], v[44:47], a[68:71], v[180:183]\n\t"
    "v_mfma_f32_16x16x32_bf16 v[184:187], v[44:47], a[72:75], v[184:187]\n\t"
    "ds_read_b128 v[92:95], v6 offset:47104\n\t"
    "v_mfma_f32_16x16x32_bf16 v[188:191], v[44:47], a[76:79], v[188:191]\n\t"
    "s_waitcnt lgkmcnt(11)\n\t"
    "v_mfma_f32_16x16x32_bf16 v[192:195], v[48:51], a[64:67], v[192:195]\n\t"
    "v_mfma_f32_16x16x32_bf16 v[196:199], v[48:51], a[68:71], v[196:199]\n\t"
    "v_mfma_f32_16x16x32_bf16 v[200:203], v[48:51], a[72:75], v[200:203]\n\t"
    "v_mfma_f32_16x16x32_bf16 v[204:207], v[48:51], a[76:79], v[204:207]\n\t"
    "s_waitcnt lgkmcnt(10)\n\t"
    "v_mfma_f32_16x16x32_bf16 v[208:211], v[52:55], a[64:67], v[208:211]\n\t"
    "v_mfma_f32_16x16x32_bf16 v[212:215], v[52:55], a[68:71], v[212:215]\n\t"
    "v_mfma_f32_16x16x32_bf16 v[216:219], v[52:55], a[72:75], v[216:219]\n\t"
    "v_mfma_f32_16x16x32_bf16 v[220:223], v[52:55], a[76:79], v[220:223]\n\t"
    "s_waitcnt lgkmcnt(9)\n\t"
    "v_mfma_f32_16x16x32_bf16 v[224:227], v[56:59], a[64:67], v[224:227]\n\t"
    "v_mfma_f32_16x16x32_bf16 v[228:231], v[56:59], a[68:71], v[228:231]\n\t"
    "v_mfma_f32_16x16x32_bf16 v[232:235], v[56:59], a[72:75], v[232:235]\n\t"
    "v_mfma_f32_16x16x32_bf16 v[236:239], v[56:59], a[76:79], v[236:239]\n\t"
    "s_waitcnt lgkmcnt(8)\n\t"
    "v_mfma_f32_16x16x32_bf16 v[240:243], v[60:63], a[64:67], v[240:243]\n\t"
    "v_mfma_f32_16x16x32_bf16 v[244:247], v[60:63], a[68:71], v[244:247]\n\t"
    "v_mfma_f32_16x16x32_bf16 v[248:251], v[60:63], a[72:75], v[248:251]\n\t"
    "v_mfma_f32_16x16x32_bf16 v[252:255], v[60:63], a[76:79], v[252:255]\n\t"
    "s_waitcnt vmcnt(24)\n\t"
    "s_waitcnt lgkmcnt(0)\n\t"
    "s_barrier\n\t"
    "v_mfma_f32_16x16x32_bf16 v[128:131], v[64:67], a[80:83], v[128:131]\n\t"
    "ds_read_b128 v[32:35], v4 offset:49152\n\t"
    "v_mfma_f32_16x16x32_bf16 v[132:135], v[64:67], a[84:87], v[132:135]\n\t"
    "v_mfma_f32_16x16x32_bf16 v[136:139], v[64:67], a[88:91], v[136:139]\n\t"
    "ds_read_b128 v[36:39], v4 offset:51200\n\t"
    "v_mfma_f32_16x16x32_bf16 v[140:143], v[64:67], a[92:95], v[140:143]\n\t"
    "s_add_u32 s46, s41, 256\n\t"
    "s_add_u32 m0, %[mb], 32768\n\t"
    "s_nop 0\n\t"
    "buffer_load_dwordx4 v0, %[rx], s46 offen lds\n\t"
    "v_mfma_f32_16x16x32_bf16 v[144:147], v[68:71], a[80:83], v[144:147]\n\t"
    "ds_read_b128 v[40:43], v4 offset:53248\n\t"
    "v_mfma_f32_16x16x32_bf16 v[148:151], v[68:71], a[84:87], v[148:151]\n\t"
    "v_mfma_f32_16x16x32_bf16 v[152:155], v[68:71], a[88:91], v[152:155]\n\t"
    "ds_read_b128 v[44:47], v4 offset:55296\n\t"
    "v_mfma_f32_16x16x32_bf16 v[156:159], v[68:71], a[92:95], v[156:159]\n\t"
    "v_mfma_f32_16x16x32_bf16 v[160:163], v[72:75], a[80:83], v[160:163]\n\t"
    "ds_read_b128 v[48:51], v4 offset:57344\n\t"
    "v_mfma_f32_16x16x32_bf16 v[164:167], v[72:75], a[84:87], v[164:167]\n\t"
    "s_add_u32 m0, %[mb], 33792\n\t"
    "s_nop 0\n\t"
    "buffer_load_dwordx4 v1, %[rx], s46 offen lds\n\t"
    "v_mfma_f32_16x16x32_bf16 v[168:171], v[72:75], a[88:91], v[168:171]\n\t"
    "ds_read_b128 v[52:55], v4 offset:59392\n\t"
    "v_mfma_f32_16x16x32_bf16 v[172:175], v[72:75], a[92:95], v[172:175]\n\t"
    "v_mfma_f32_16x16x32_bf16 v[176:179], v[76:79], a[80:83], v[176:179]\n\t"
    "ds_read_b128 v[56:59], v4 offset:61440\n\t"
    "v_mfma_f32_16x16x32_bf16 v[180:183], v[76:79], a[84:87], v[180:183]\n\t"
    "v_mfma_f32_16x16x32_bf16 v[184:187], v[76:79], a[88:91], v[184:187]\n\t"
    "ds_read_b128 v[60:63], v4 offset:63488\n\t"
    "v_mfma_f32_16x16x32_bf16 v[188:191], v[76:79], a[92:95], v[188:191]\n\t"
    "s_add_u32 m0, %[mb], 34816\n\t"
    "s_nop 0\n\t"
    "buffer_load_dwordx4 v2, %[rx], s46 offen lds\n\t"
    "v_mfma_f32_16x16x32_bf16 v[192:195], v[80:83], a[80:83], v[192:195]\n\t"
    "v_mfma_f32_16x16x32_bf16 v[196:199], v[80:83], a[84:87], v[196:199]\n\t"
    "v_mfma_f32_16x16x32_bf16 v[200:203], v[80:83], a[88:91], v[200:203]\n\t"
    "v_mfma_f32_16x16x32_bf16 v[204:207], v[80:83], a[92:95], v[204:207]\n\t"
    "v_mfma_f32_16x16x32_bf16 v[208:211], v[84:87], a[80:83], v[208:211]\n\t"
    "v_mfma_f32_16x16x32_bf16 v[212:215], v[84:87], a[84:87], v[212:215]\n\t"
    "s_add_u32 m0, %[mb], 35840\n\t"
    "s_nop 0\n\t"
    "buffer_load_dwordx4 v3, %[rx], s46 offen lds\n\t"
    "v_mfma_f32_16x16x32_bf16 v[216:219], v[84:87], a[88:91], v[216:219]\n\t"
    "v_mfma_f32_16x16x32_bf16 v[220:223], v[84:87], a[92:95], v[220:223]\n\t"
    "v_mfma_f32_16x16x32_bf16 v[224:227], v[88:91], a[80:83], v[224:227]\n\t"
    "v_mfma_f32_16x16x32_bf16 v[228:231], v[88:91], a[84:87], v[228:231]\n\t"
    "v_mfma_f32_16x16x32_bf16 v[232:235], v[88:91], a[88:91], v[232:235]\n\t"
    "v_mfma_f32_16x16x32_bf16 v[236:239], v[88:91], a[92:95], v[236:239]\n\t"
    "v_mfma_f32_16x16x32_bf16 v[240:243], v[92:95], a[80:83], v[240:243]\n\t"
    "v_mfma_f32_16x16x32_bf16 v[244:247], v[92:95], a[84:87], v[244:247]\n\t"
    "v_mfma_f32_16x16x32_bf16 v[248:251], v[92:95], a[88:91], v[248:251]\n\t"
    "v_mfma_f32_16x16x32_bf16 v[252:255], v[92:95], a[92:95], v[252:255]\n\t"
    "s_waitcnt lgkmcnt(7)\n\t"
    "v_mfma_f32_16x16x32_bf16 v[128:131], v[32:35], a[96:99], v[128:131]\n\t"
    "ds_read_b128 v[64:67], v6 offset:49152\n\t"
    "v_mfma_f32_16x16x32_bf16 v[132:135], v[32:35], a[100:103], v[132:135]\n\t"
    "v_mfma_f32_16x16x32_bf16 v[136:139], v[32:35], a[104:107], v[136:139]\n\t"
    "ds_read_b128 v[68:71], v6 offset:51200\n\t"
    "v_mfma_f32_16x16x32_bf16 v[140:143], v[32:35], a[108:111], v[140:143]\n\t"
    "s_waitcnt lgkmcnt(8)\n\t"
    "v_mfma_f32_16x16x32_bf16 v[144:147], v[36:39], a[96:99], v[144:147]\n\t"
    "ds_read_b128 v[72:75], v6 offset:53248\n\t"
    "v_mfma_f32_16x16x32_bf16 v[148:151], v[36:39], a[100:103], v[148:151]\n\t"
    "v_mfma_f32_16x16x32_bf16 v[152:155], v[36:39], a[104:107], v[152:155]\n\t"
    "ds_read_b128 v[76:79], v6 offset:55296\n\t"
    "v_mfma_f32_16x16x32_bf16 v[156:159], v[36:39], a[108:111], v[156:159]\n\t"
    "s_waitcnt lgkmcnt(9)\n\t"
    "v_mfma_f32_16x16x32_bf16 v[160:163], v[40:43], a[96:99], v[160:163]\n\t"
    "ds_read_b128 v[80:83], v6 offset:57344\n\t"
    "v_mfma_f32_16x16x32_bf16 v[164:167], v[40:43], a[100:103], v[164:167]\n\t"
    "v_mfma_f32_16x16x32_bf16 v[168:171], v[40:43], a[104:107], v[168:171]\n\t"
    "ds_read_b128 v[84:87], v6 offset:59392\n\t"
    "v_mfma_f32_16x16x32_bf16 v[172:175], v[40:43], a[108:111], v[172:175]\n\t"
    "s_waitcnt lgkmcnt(10)\n\t"
    "v_mfma_f32_16x16x32_bf16 v[176:179], v[44:47], a[96:99], v[176:179]\n\t"
    "ds_read_b128 v[88:91], v6 offset:61440\n\t"
    "v_mfma_f32_16x16x32_bf16 v[180:183], v[44:47], a[100:103], v[180:183]\n\t"
    "v_mfma_f32_16x16x32_bf16 v[184:187], v[44:47], a[104:107], v[184:187]\n\t"
    "ds_read_b128 v[92:95], v6 offset:63488\n\t"
    "v_mfma_f32_16x16x32_bf16 v[188:191], v[44:47], a[108:111], v[188:191]\n\t"
    "s_waitcnt lgkmcnt(11)\n\t"
    "v_mfma_f32_16x16x32_bf16 v[192:195], v[48:51], a[96:99], v[192:195]\n\t"
    "v_mfma_f32_16x16x32_bf16 v[196:199], v[48:51], a[100:103], v[196:199]\n\t"
    "v_mfma_f32_16x16x32_bf16 v[200:203], v[48:51], a[104:107], v[200:203]\n\t"
    "v_mfma_f32_16x16x32_bf16 v[204:207], v[48:51], a[108:111], v[204:207]\n\t"
    "s_waitcnt lgkmcnt(10)\n\t"
    "v_mfma_f32_16x16x32_bf16 v[208:211], v[52:55], a[96:99], v[208:211]\n\t"
    "v_mfma_f32_16x16x32_bf16 v[212:215], v[52:55], a[100:103], v[212:215]\n\t"
    "v_mfma_f32_16x16x32_bf16 v[216:219], v[52:55], a[104:107], v[216:219]\n\t"
    "v_mfma_f32_16x16x32_bf16 v[220:223], v[52:55], a[108:111], v[220:223]\n\t"
    "s_waitcnt lgkmcnt(9)\n\t"
    "v_mfma_f32_16x16x32_bf16 v[224:227], v[56:59], a[96:99], v[224:227]\n\t"
    "v_mfma_f32_16x16x32_bf16 v[228:231], v[56:59], a[100:103], v[228:231]\n\t"
    "v_mfma_f32_16x16x32_bf16 v[232:235], v[56:59], a[104:107], v[232:235]\n\t"
    "v_mfma_f32_16x16x32_bf16 v[236:239], v[56:59], a[108:111], v[236:239]\n\t"
    "s_waitcnt lgkmcnt(8)\n\t"
    "v_mfma_f32_16x16x32_bf16 v[240:243], v[60:63], a[96:99], v[240:243]\n\t"
    "v_mfma_f32_16x16x32_bf16 v[244:247], v[60:63], a[100:103], v[244:247]\n\t"
    "v_mfma_f32_16x16x32_bf16 v[248:251], v[60:63], a[104:107], v[248:251]\n\t"
    "v_mfma_f32_16x16x32_bf16 v[252:255], v[60:63], a[108:111], v[252:255]\n\t"
    "s_waitcnt vmcnt(24)\n\t"
    "s_waitcnt lgkmcnt(0)\n\t"
    "s_barrier\n\t"
    "v_mfma_f32_16x16x32_bf16 v[128:131], v[64:67], a[112:115], v[128:131]\n\t"
    "ds_read_b128 v[32:35], v5 offset:0\n\t"
    "v_mfma_f32_16x16x32_bf16 v[132:135], v[64:67], a[116:119], v[132:135]\n\t"
    "v_mfma_f32_16x16x32_bf16 v[136:139], v[64:67], a[120:123], v[136:139]\n\t"
    "ds_read_b128 v[36:39], v5 offset:2048\n\t"
    "v_mfma_f32_16x16x32_bf16 v[140:143], v[64:67], a[124:127], v[140:143]\n\t"
    "s_add_u32 s46, s41, 384\n\t"
    "s_add_u32 m0, %[mb], 49152\n\t"
    "s_nop 0\n\t"
    "buffer_load_dwordx4 v0, %[rx], s46 offen lds\n\t"
    "v_mfma_f32_16x16x32_bf16 v[144:147], v[68:71], a[112:115], v[144:147]\n\t"
    "ds_read_b128 v[40:43], v5 offset:4096\n\t"
    "v_mfma_f32_16x16x32_bf16 v[148:151], v[68:71], a[116:119], v[148:151]\n\t"
    "v_mfma_f32_16x16x32_bf16 v[152:155], v[68:71], a[120:123], v[152:155]\n\t"
    "ds_read_b128 v[44:47], v5 offset:6144\n\t"
    "v_mfma_f32_16x16x32_bf16 v[156:159], v[68:71], a[124:127], v[156:159]\n\t"
    "v_mfma_f32_16x16x32_bf16 v[160:163], v[72:75], a[112:115], v[160:163]\n\t"
    "ds_read_b128 v[48:51], v5 offset:8192\n\t"
    "v_mfma_f32_16x16x32_bf16 v[164:167], v[72:75], a[116:119], v[164:167]\n\t"
    "s_add_u32 m0, %[mb], 50176\n\t"
    "s_nop 0\n\t"
    "buffer_load_dwordx4 v1, %[rx], s46 offen lds\n\t"
    "v_mfma_f32_16x16x32_bf16 v[168:171], v[72:75], a[120:123], v[168:171]\n\t"
    "ds_read_b128 v[52:55], v5 offset:10240\n\t"
    "v_mfma_f32_16x16x32_bf16 v[172:175], v[72:75], a[124:127], v[172:175]\n\t"
    "v_mfma_f32_16x16x32_bf16 v[176:179], v[76:79], a[112:115], v[176:179]\n\t"
    "ds_read_b128 v[56:59], v5 offset:12288\n\t"
    "v_mfma_f32_16x16x32_bf16 v[180:183], v[76:79], a[116:119], v[180:183]\n\t"
    "v_mfma_f32_16x16x32_bf16 v[184:187], v[76:79], a[120:123], v[184:187]\n\t"
    "ds_read_b128 v[60:63], v5 offset:14336\n\t"
    "v_mfma_f32_16x16x32_bf16 v[188:191], v[76:79], a[124:127], v[188:191]\n\t"
    "s_add_u32 m0, %[mb], 51200\n\t"
    "s_nop 0\n\t"
    "buffer_load_dwordx4 v2, %[rx], s46 offen lds\n\t"
    "v_mfma_f32_16x16x32_bf16 v[192:195], v[80:83], a[112:115], v[192:195]\n\t"
    "v_mfma_f32_16x16x32_bf16 v[196:199], v[80:83], a[116:119], v[196:199]\n\t"
    "v_mfma_f32_16x16x32_bf16 v[200:203], v[80:83], a[120:123], v[200:203]\n\t"
    "v_mfma_f32_16x16x32_bf16 v[204:207], v[80:83], a[124:127], v[204:207]\n\t"
    "v_mfma_f32_16x16x32_bf16 v[208:211], v[84:87], a[112:115], v[208:211]\n\t"
    "v_mfma_f32_16x16x32_bf16 v[212:215], v[84:87], a[116:119], v[212:215]\n\t"
    "s_add_u32 m0, %[mb], 52224\n\t"
    "s_nop 0\n\t"
    "buffer_load_dwordx4 v3, %[rx], s46 offen lds\n\t"
    "v_mfma_f32_16x16x32_bf16 v[216:219], v[84:87], a[120:123], v[216:219]\n\t"
    "v_mfma_f32_16x16x32_bf16 v[220:223], v[84:87], a[124:127], v[220:223]\n\t"
    "v_mfma_f32_16x16x32_bf16 v[224:227], v[88:91], a[112:115], v[224:227]\n\t"
    "v_mfma_f32_16x16x32_bf16 v[228:231], v[88:91], a[116:119], v[228:231]\n\t"
    "v_mfma_f32_16x16x32_bf16 v[232:235], v[88:91], a[120:123], v[232:235]\n\t"
    "v_mfma_f32_16x16x32_bf16 v[236:239], v[88:91], a[124:127], v[236:239]\n\t"
    "v_mfma_f32_16x16x32_bf16 v[240:243], v[92:95], a[112:115], v[240:243]\n\t"
    "v_mfma_f32_16x16x32_bf16 v[244:247], v[92:95], a[116:119], v[244:247]\n\t"
    "v_mfma_f32_16x16x32_bf16 v[248:251], v[92:95], a[120:123], v[248:251]\n\t"
    "v_mfma_f32_16x16x32_bf16 v[252:255], v[92:95], a[124:127], v[252:255]\n\t"
    "s_waitcnt lgkmcnt(7)\n\t"
    "v_mfma_f32_16x16x32_bf16 v[128:131], v[32:35], a[128:131], v[128:131]\n\t"
    "ds_read_b128 v[64:67], v7 offset:0\n\t"
    "v_mfma_f32_16x16x32_bf16 v[132:135], v[32:35], a[132:135], v[132:135]\n\t"
    "v_mfma_f32_16x16x32_bf16 v[136:139], v[32:35], a[136:139], v[136:139]\n\t"
    "ds_read_b128 v[68:71], v7 offset:2048\n\t"
    "v_mfma_f32_16x16x32_bf16 v[140:143], v[32:35], a[140:143], v[140:143]\n\t"
    "s_waitcnt lgkmcnt(8)\n\t"
    "v_mfma_f32_16x16x32_bf16 v[144:147], v[36:39], a[128:131], v[144:147]\n\t"
    "ds_read_b128 v[72:75], v7 offset:4096\n\t"
    "v_mfma_f32_16x16x32_bf16 v[148:151], v[36:39], a[132:135], v[148:151]\n\t"
    "v_mfma_f32_16x16x32_bf16 v[152:155], v[36:39], a[136:139], v[152:155]\n\t"
    "ds_read_b128 v[76:79], v7 offset:6144\n\t"
    "v_mfma_f32_16x16x32_bf16 v[156:159], v[36:39], a[140:143], v[156:159]\n\t"
    "s_waitcnt lgkmcnt(9)\n\t"
    "v_mfma_f32_16x16x32_bf16 v[160:163], v[40:43], a[128:131], v[160:163]\n\t"
    "ds_read_b128 v[80:83], v7 offset:8192\n\t"
    "v_mfma_f32_16x16x32_bf16 v[164:167], v[40:43], a[132:135], v[164:167]\n\t"
    "v_mfma_f32_16x16x32_bf16 v[168:171], v[40:43], a[136:139], v[168:171]\n\t"
    "ds_read_b128 v[84:87], v7 offset:10240\n\t"
    "v_mfma_f32_16x16x32_bf16 v[172:175], v[40:43], a[140:143], v[172:175]\n\t"
    "s_waitcnt lgkmcnt(10)\n\t"
    "v_mfma_f32_16x16x32_bf16 v[176:179], v[44:47], a[128:131], v[176:179]\n\t"
    "ds_read_b128 v[88:91], v7 offset:12288\n\t"
    "v_mfma_f32_16x16x32_bf16 v[180:183], v[44:47], a[132:135], v[180:183]\n\t"
    "v_mfma_f32_16x16x32_bf16 v[184:187], v[44:47], a[136:139], v[184:187]\n\t"
    "ds_read_b128 v[92:95], v7 offset:14336\n\t"
    "v_mfma_f32_16x16x32_bf16 v[188:191], v[44:47], a[140:143], v[188:191]\n\t"
    "s_waitcnt lgkmcnt(11)\n\t"
    "v_mfma_f32_16x16x32_bf16 v[192:195], v[48:51], a[128:131], v[192:195]\n\t"
    "v_mfma_f32_16x16x32_bf16 v[196:199], v[48:51], a[132:135], v[196:199]\n\t"
    "v_mfma_f32_16x16x32_bf16 v[200:203], v[48:51], a[136:139], v[200:203]\n\t"
    "v_mfma_f32_16x16x32_bf16 v[204:207], v[48:51], a[140:143], v[204:207]\n\t"
    "s_waitcnt lgkmcnt(10)\n\t"
    "v_mfma_f32_16x16x32_bf16 v[208:211], v[52:55], a[128:131], v[208:211]\n\t"
    "v_mfma_f32_16x16x32_bf16 v[212:215], v[52:55], a[132:135], v[212:215]\n\t"
    "v_mfma_f32_16x16x32_bf16 v[216:219], v[52:55], a[136:139], v[216:219]\n\t"
    "v_mfma_f32_16x16x32_bf16 v[220:223], v[52:55], a[140:143], v[220:223]\n\t"
    "s_waitcnt lgkmcnt(9)\n\t"
    "v_mfma_f32_16x16x32_bf16 v[224:227], v[56:59], a[128:131], v[224:227]\n\t"
    "v_mfma_f32_16x16x32_bf16 v[228:231], v[56:59], a[132:135], v[228:231]\n\t"
    "v_mfma_f32_16x16x32_bf16 v[232:235], v[56:59], a[136:139], v[232:235]\n\t"
    "v_mfma_f32_16x16x32_bf16 v[236:239], v[56:59], a[140:143], v[236:239]\n\t"
    "s_waitcnt lgkmcnt(8)\n\t"
    "v_mfma_f32_16x16x32_bf16 v[240:243], v[60:63], a[128:131], v[240:243]\n\t"
    "v_mfma_f32_16x16x32_bf16 v[244:247], v[60:63], a[132:135], v[244:247]\n\t"
    "v_mfma_f32_16x16x32_bf16 v[248:251], v[60:63], a[136:139], v[248:251]\n\t"
    "v_mfma_f32_16x16x32_bf16 v[252:255], v[60:63], a[140:143], v[252:255]\n\t"
    "s_waitcnt vmcnt(24)\n\t"
    "s_waitcnt lgkmcnt(0)\n\t"
    "s_barrier\n\t"
    "v_mfma_f32_16x16x32_bf16 v[128:131], v[64:67], a[144:147], v[128:131]\n\t"
    "ds_read_b128 v[32:35], v5 offset:16384\n\t"
    "v_mfma_f32_16x16x32_bf16 v[132:135], v[64:67], a[148:151], v[132:135]\n\t"
    "v_mfma_f32_16x16x32_bf16 v[136:139], v[64:67], a[152:155], v[136:139]\n\t"
    "ds_read_b128 v[36:39], v5 offset:18432\n\t"
    "v_mfma_f32_16x16x32_bf16 v[140:143], v[64:67], a[156:159], v[140:143]\n\t"
    "s_add_u32 s46, s41, 512\n\t"
    "s_add_u32 m0, %[mb], 65536\n\t"
    "s_nop 0\n\t"
    "buffer_load_dwordx4 v0, %[rx], s46 offen lds\n\t"
    "v_mfma_f32_16x16x32_bf16 v[144:147], v[68:71], a[144:147], v[144:147]\n\t"
    "ds_read_b128 v[40:43], v5 offset:20480\n\t"
    "v_mfma_f32_16x16x32_bf16 v[148:151], v[68:71], a[148:151], v[148:151]\n\t"
    "v_mfma_f32_16x16x32_bf16 v[152:155], v[68:71], a[152:155], v[152:155]\n\t"
    "ds_read_b128 v[44:47], v5 offset:22528\n\t"
    "v_mfma_f32_16x16x32_bf16 v[156:159], v[68:71], a[156:159], v[156:159]\n\t"
    "v_mfma_f32_16x16x32_bf16 v[160:163], v[72:75], a[144:147], v[160:163]\n\t"
    "ds_read_b128 v[48:51], v5 offset:24576\n\t"
    "v_mfma_f32_16x16x32_bf16 v[164:167], v[72:75], a[148:151], v[164:167]\n\t"
    "s_add_u32 m0, %[mb], 66560\n\t"
    "s_nop 0\n\t"
    "buffer_load_dwordx4 v1, %[rx], s46 offen lds\n\t"
    "v_mfma_f32_16x16x32_bf16 v[168:171], v[72:75], a[152:155], v[168:171]\n\t"
    "ds_read_b128 v[52:55], v5 offset:26624\n\t"
    "v_mfma_f32_16x16x32_bf16 v[172:175], v[72:75], a[156:159], v[172:175]\n\t"
    "v_mfma_f32_16x16x32_bf16 v[176:179], v[76:79], a[144:147], v[176:179]\n\t"
    "ds_read_b128 v[56:59], v5 offset:28672\n\t"
    "v_mfma_f32_16x16x32_bf16 v[180:183], v[76:79], a[148:151], v[180:183]\n\t"
    "v_mfma_f32_16x16x32_bf16 v[184:187], v[76:79], a[152:155], v[184:187]\n\t"
    "ds_read_b128 v[60:63], v5 offset:30720\n\t"
    "v_mfma_f32_16x16x32_bf16 v[188:191], v[76:79], a[156:159], v[188:191]\n\t"
    "s_add_u32 m0, %[mb], 67584\n\t"
    "s_nop 0\n\t"
    "buffer_load_dwordx4 v2, %[rx], s46 offen lds\n\t"
    "v_mfma_f32_16x16x32_bf16 v[192:195], v[80:83], a[144:147], v[192:195]\n\t"
    "v_mfma_f32_16x16x32_bf16 v[196:199], v[80:83], a[148:151], v[196:199]\n\t"
    "v_mfma_f32_16x16x32_bf16 v[200:203], v[80:83], a[152:155], v[200:203]\n\t"
    "v_mfma_f32_16x16x32_bf16 v[204:207], v[80:83], a[156:159], v[204:207]\n\t"
    "v_mfma_f32_16x16x32_bf16 v[208:211], v[84:87], a[144:147], v[208:211]\n\t"
    "v_mfma_f32_16x16x32_bf16 v[212:215], v[84:87], a[148:151], v[212:215]\n\t"
    "s_add_u32 m0, %[mb], 68608\n\t"
    "s_nop 0\n\t"
    "buffer_load_dwordx4 v3, %[rx], s46 offen lds\n\t"
    "v_mfma_f32_16x16x32_bf16 v[216:219], v[84:87], a[152:155], v[216:219]\n\t"
    "v_mfma_f32_16x16x32_bf16 v[220:223], v[84:87], a[156:159], v[220:223]\n\t"
    "v_mfma_f32_16x16x32_bf16 v[224:227], v[88:91], a[144:147], v[224:227]\n\t"
    "v_mfma_f32_16x16x32_bf16 v[228:231], v[88:91], a[148:151], v[228:231]\n\t"
    "v_mfma_f32_16x16x32_bf16 v[232:235], v[88:91], a[152:155], v[232:235]\n\t"
    "v_mfma_f32_16x16x32_bf16 v[236:239], v[88:91], a[156:159], v[236:239]\n\t"
    "v_mfma_f32_16x16x32_bf16 v[240:243], v[92:95], a[144:147], v[240:243]\n\t"
    "v_mfma_f32_16x16x32_bf16 v[244:247], v[92:95], a[148:151], v[244:247]\n\t"
    "v_mfma_f32_16x16x32_bf16 v[248:251], v[92:95], a[152:155], v[248:251]\n\t"
    "v_mfma_f32_16x16x32_bf16 v[252:255], v[92:95], a[156:159], v[252:255]\n\t"
    "s_waitcnt lgkmcnt(7)\n\t"
    "v_mfma_f32_16x16x32_bf16 v[128:131], v[32:35], a[160:163], v[128:131]\n\t"
    "ds_read_b128 v[64:67], v7 offset:16384\n\t"
    "v_mfma_f32_16x16x32_bf16 v[132:135], v[32:35], a[164:167], v[132:135]\n\t"
    "v_mfma_f32_16x16x32_bf16 v[136:139], v[32:35], a[168:171], v[136:139]\n\t"
    "ds_read_b128 v[68:71], v7 offset:18432\n\t"
    "v_mfma_f32_16x16x32_bf16 v[140:143], v[32:35], a[172:175], v[140:143]\n\t"
    "s_waitcnt lgkmcnt(8)\n\t"
    "v_mfma_f32_16x16x32_bf16 v[144:147], v[36:39], a[160:163], v[144:147]\n\t"
    "ds_read_b128 v[72:75], v7 offset:20480\n\t"
    "v_mfma_f32_16x16x32_bf16 v[148:151], v[36:39], a[164:167], v[148:151]\n\t"
    "v_mfma_f32_16x16x32_bf16 v[152:155], v[36:39], a[168:171], v[152:155]\n\t"
    "ds_read_b128 v[76:79], v7 offset:22528\n\t"
    "v_mfma_f32_16x16x32_bf16 v[156:159], v[36:39], a[172:175], v[156:159]\n\t"
    "s_waitcnt lgkmcnt(9)\n\t"
    "v_mfma_f32_16x16x32_bf16 v[160:163], v[40:43], a[160:163], v[160:163]\n\t"
    "ds_read_b128 v[80:83], v7 offset:24576\n\t"
    "v_mfma_f32_16x16x32_bf16 v[164:167], v[40:43], a[164:167], v[164:167]\n\t"
    "v_mfma_f32_16x16x32_bf16 v[168:171], v[40:43], a[168:171], v[168:171]\n\t"
    "ds_read_b128 v[84:87], v7 offset:26624\n\t"
    "v_mfma_f32_16x16x32_bf16 v[172:175], v[40:43], a[172:175], v[172:175]\n\t"
    "s_waitcnt lgkmcnt(10)\n\t"
    "v_mfma_f32_16x16x32_bf16 v[176:179], v[44:47], a[160:163], v[176:179]\n\t"
    "ds_read_b128 v[88:91], v7 offset:28672\n\t"
    "v_mfma_f32_16x16x32_bf16 v[180:183], v[44:47], a[164:167], v[180:183]\n\t"
    "v_mfma_f32_16x16x32_bf16 v[184:187], v[44:47], a[168:171], v[184:187]\n\t"
    "ds_read_b128 v[92:95], v7 offset:30720\n\t"
    "v_mfma_f32_16x16x32_bf16 v[188:191], v[44:47], a[172:175], v[188:191]\n\t"
    "s_waitcnt lgkmcnt(11)\n\t"
    "v_mfma_f32_16x16x32_bf16 v[192:195], v[48:51], a[160:163], v[192:195]\n\t"
    "v_mfma_f32_16x16x32_bf16 v[196:199], v[48:51], a[164:167], v[196:199]\n\t"
    "v_mfma_f32_16x16x32_bf16 v[200:203], v[48:51], a[168:171], v[200:203]\n\t"
    "v_mfma_f32_16x16x32_bf16 v[204:207], v[48:51], a[172:175], v[204:207]\n\t"
    "s_waitcnt lgkmcnt(10)\n\t"
    "v_mfma_f32_16x16x32_bf16 v[208:211], v[52:55], a[160:163], v[208:211]\n\t"
    "v_mfma_f32_16x16x32_bf16 v[212:215], v[52:55], a[164:167], v[212:215]\n\t"
    "v_mfma_f32_16x16x32_bf16 v[216:219], v[52:55], a[168:171], v[216:219]\n\t"
    "v_mfma_f32_16x16x32_bf16 v[220:223], v[52:55], a[172:175], v[220:223]\n\t"
    "s_waitcnt lgkmcnt(9)\n\t"
    "v_mfma_f32_16x16x32_bf16 v[224:227], v[56:59], a[160:163], v[224:227]\n\t"
    "v_mfma_f32_16x16x32_bf16 v[228:231], v[56:59], a[164:167], v[228:231]\n\t"
    "v_mfma_f32_16x16x32_bf16 v[232:235], v[56:59], a[168:171], v[232:235]\n\t"
    "v_mfma_f32_16x16x32_bf16 v[236:239], v[56:59], a[172:175], v[236:239]\n\t"
    "s_waitcnt lgkmcnt(8)\n\t"
    "v_mfma_f32_16x16x32_bf16 v[240:243], v[60:63], a[160:163], v[240:243]\n\t"
    "v_mfma_f32_16x16x32_bf16 v[244:247], v[60:63], a[164:167], v[244:247]\n\t"
    "v_mfma_f32_16x16x32_bf16 v[248:251], v[60:63], a[168:171], v[248:251]\n\t"
    "v_mfma_f32_16x16x32_bf16 v[252:255], v[60:63], a[172:175], v[252:255]\n\t"
    "s_waitcnt vmcnt(24)\n\t"
    "s_waitcnt lgkmcnt(0)\n\t"
    "s_barrier\n\t"
    "v_mfma_f32_16x16x32_bf16 v[128:131], v[64:67], a[176:179], v[128:131]\n\t"
    "ds_read_b128 v[32:35], v5 offset:32768\n\t"
    "v_mfma_f32_16x16x32_bf16 v[132:135], v[64:67], a[180:183], v[132:135]\n\t"
    "v_mfma_f32_16x16x32_bf16 v[136:139], v[64:67], a[184:187], v[136:139]\n\t"
    "ds_read_b128 v[36:39], v5 offset:34816\n\t"
    "v_mfma_f32_16x16x32_bf16 v[140:143], v[64:67], a[188:191], v[140:143]\n\t"
    "s_add_u32 s46, s41, 640\n\t"
    "s_add_u32 m0, %[mb], 81920\n\t"
    "s_nop 0\n\t"
    "buffer_load_dwordx4 v0, %[rx], s46 offen lds\n\t"
    "v_mfma_f32_16x16x32_bf16 v[144:147], v[68:71], a[176:179], v[144:147]\n\t"
    "ds_read_b128 v[40:43], v5 offset:36864\n\t"
    "v_mfma_f32_16x16x32_bf16 v[148:151], v[68:71], a[180:183], v[148:151]\n\t"
    "v_mfma_f32_16x16x32_bf16 v[152:155], v[68:71], a[184:187], v[152:155]\n\t"
    "ds_read_b128 v[44:47], v5 offset:38912\n\t"
    "v_mfma_f32_16x16x32_bf16 v[156:159], v[68:71], a[188:191], v[156:159]\n\t"
    "v_mfma_f32_16x16x32_bf16 v[160:163], v[72:75], a[176:179], v[160:163]\n\t"
    "ds_read_b128 v[48:51], v5 offset:40960\n\t"
    "v_mfma_f32_16x16x32_bf16 v[164:167], v[72:75], a[180:183], v[164:167]\n\t"
    "s_add_u32 m0, %[mb], 82944\n\t"
    "s_nop 0\n\t"
    "buffer_load_dwordx4 v1, %[rx], s46 offen lds\n\t"
    "v_mfma_f32_16x16x32_bf16 v[168:171], v[72:75], a[184:187], v[168:171]\n\t"
    "ds_read_b128 v[52:55], v5 offset:43008\n\t"
    "v_mfma_f32_16x16x32_bf16 v[172:175], v[72:75], a[188:191], v[172:175]\n\t"
    "v_mfma_f32_16x16x32_bf16 v[176:179], v[76:79], a[176:179], v[176:179]\n\t"
    "ds_read_b128 v[56:59], v5 offset:45056\n\t"
    "v_mfma_f32_16x16x32_bf16 v[180:183], v[76:79], a[180:183], v[180:183]\n\t"
    "v_mfma_f32_16x16x32_bf16 v[184:187], v[76:79], a[184:187], v[184:187]\n\t"
    "ds_read_b128 v[60:63], v5 offset:47104\n\t"
    "v_mfma_f32_16x16x32_bf16 v[188:191], v[76:79], a[188:191], v[188:191]\n\t"
    "s_add_u32 m0, %[mb], 83968\n\t"
    "s_nop 0\n\t"
    "buffer_load_dwordx4 v2, %[rx], s46 offen lds\n\t"
    "v_mfma_f32_16x16x32_bf16 v[192:195], v[80:83], a[176:179], v[192:195]\n\t"
    "v_mfma_f32_16x16x32_bf16 v[196:199], v[80:83], a[180:183], v[196:199]\n\t"
    "v_mfma_f32_16x16x32_bf16 v[200:203], v[80:83], a[184:187], v[200:203]\n\t"
    "v_mfma_f32_16x16x32_bf16 v[204:207], v[80:83], a[188:191], v[204:207]\n\t"
    "v_mfma_f32_16x16x32_bf16 v[208:211], v[84:87], a[176:179], v[208:211]\n\t"
    "v_mfma_f32_16x16x32_bf16 v[212:215], v[84:87], a[180:183], v[212:215]\n\t"
    "s_add_u32 m0, %[mb], 84992\n\t"
    "s_nop 0\n\t"
    "buffer_load_dwordx4 v3, %[rx], s46 offen lds\n\t"
    "v_mfma_f32_16x16x32_bf16 v[216:219], v[84:87], a[184:187], v[216:219]\n\t"
    "v_mfma_f32_16x16x32_bf16 v[220:223], v[84:87], a[188:191], v[220:223]\n\t"
    "v_mfma_f32_16x16x32_bf16 v[224:227], v[88:91], a[176:179], v[224:227]\n\t"
    "v_mfma_f32_16x16x32_bf16 v[228:231], v[88:91], a[180:183], v[228:231]\n\t"
    "v_mfma_f32_16x16x32_bf16 v[232:235], v[88:91], a[184:187], v[232:235]\n\t"
    "v_mfma_f32_16x16x32_bf16 v[236:239], v[88:91], a[188:191], v[236:239]\n\t"
    "v_mfma_f32_16x16x32_bf16 v[240:243], v[92:95], a[176:179], v[240:243]\n\t"
    "v_mfma_f32_16x16x32_bf16 v[244:247], v[92:95], a[180:183], v[244:247]\n\t"
    "v_mfma_f32_16x16x32_bf16 v[248:251], v[92:95], a[184:187], v[248:251]\n\t"
    "v_mfma_f32_16x16x32_bf16 v[252:255], v[92:95], a[188:191], v[252:255]\n\t"
    "s_waitcnt lgkmcnt(7)\n\t"
    "v_mfma_f32_16x16x32_bf16 v[128:131], v[32:35], a[192:195], v[128:131]\n\t"
    "ds_read_b128 v[64:67], v7 offset:32768\n\t"
    "v_mfma_f32_16x16x32_bf16 v[132:135], v[32:35], a[196:199], v[132:135]\n\t"
    "v_mfma_f32_16x16x32_bf16 v[136:139], v[32:35], a[200:203], v[136:139]\n\t"
    "ds_read_b128 v[68:71], v7 offset:34816\n\t"
    "v_mfma_f32_16x16x32_bf16 v[140:143], v[32:35], a[204:207], v[140:143]\n\t"
    "s_waitcnt lgkmcnt(8)\n\t"
    "v_mfma_f32_16x16x32_bf16 v[144:147], v[36:39], a[192:195], v[144:147]\n\t"
    "ds_read_b128 v[72:75], v7 offset:36864\n\t"
    "v_mfma_f32_16x16x32_bf16 v[148:151], v[36:39], a[196:199], v[148:151]\n\t"
    "v_mfma_f32_16x16x32_bf16 v[152:155], v[36:39], a[200:203], v[152:155]\n\t"
    "ds_read_b128 v[76:79], v7 offset:38912\n\t"
    "v_mfma_f32_16x16x32_bf16 v[156:159], v[36:39], a[204:207], v[156:159]\n\t"
    "s_waitcnt lgkmcnt(9)\n\t"
    "v_mfma_f32_16x16x32_bf16 v[160:163], v[40:43], a[192:195], v[160:163]\n\t"
    "ds_read_b128 v[80:83], v7 offset:40960\n\t"
    "v_mfma_f32_16x16x32_bf16 v[164:167], v[40:43], a[196:199], v[164:167]\n\t"
    "v_mfma_f32_16x16x32_bf16 v[168:171], v[40:43], a[200:203], v[168:171]\n\t"
    "ds_read_b128 v[84:87], v7 offset:43008\n\t"
    "v_mfma_f32_16x16x32_bf16 v[172:175], v[40:43], a[204:207], v[172:175]\n\t"
    "s_waitcnt lgkmcnt(10)\n\t"
    "v_mfma_f32_16x16x32_bf16 v[176:179], v[44:47], a[192:195], v[176:179]\n\t"
    "ds_read_b128 v[88:91], v7 offset:45056\n\t"
    "v_mfma_f32_16x16x32_bf16 v[180:183], v[44:47], a[196:199], v[180:183]\n\t"
    "v_mfma_f32_16x16x32_bf16 v[184:187], v[44:47], a[200:203], v[184:187]\n\t"
    "ds_read_b128 v[92:95], v7 offset:47104\n\t"
    "v_mfma_f32_16x16x32_bf16 v[188:191], v[44:47], a[204:207], v[188:191]\n\t"
    "s_waitcnt lgkmcnt(11)\n\t"
    "v_mfma_f32_16x16x32_bf16 v[192:195], v[48:51], a[192:195], v[192:195]\n\t"
    "v_mfma_f32_16x16x32_bf16 v[196:199], v[48:51], a[196:199], v[196:199]\n\t"
    "v_mfma_f32_16x16x32_bf16 v[200:203], v[48:51], a[200:203], v[200:203]\n\t"
    "v_mfma_f32_16x16x32_bf16 v[204:207], v[48:51], a[204:207], v[204:207]\n\t"
    "s_waitcnt lgkmcnt(10)\n\t"
    "v_mfma_f32_16x16x32_bf16 v[208:211], v[52:55], a[192:195], v[208:211]\n\t"
    "v_mfma_f32_16x16x32_bf16 v[212:215], v[52:55], a[196:199], v[212:215]\n\t"
    "v_mfma_f32_16x16x32_bf16 v[216:219], v[52:55], a[200:203], v[216:219]\n\t"
    "v_mfma_f32_16x16x32_bf16 v[220:223], v[52:55], a[204:207], v[220:223]\n\t"
    "s_waitcnt lgkmcnt(9)\n\t"
    "v_mfma_f32_16x16x32_bf16 v[224:227], v[56:59], a[192:195], v[224:227]\n\t"
    "v_mfma_f32_16x16x32_bf16 v[228:231], v[56:59], a[196:199], v[228:231]\n\t"
    "v_mfma_f32_16x16x32_bf16 v[232:235], v[56:59], a[200:203], v[232:235]\n\t"
    "v_mfma_f32_16x16x32_bf16 v[236:239], v[56:59], a[204:207], v[236:239]\n\t"
    "s_waitcnt lgkmcnt(8)\n\t"
    "v_mfma_f32_16x16x32_bf16 v[240:243], v[60:63], a[192:195], v[240:243]\n\t"
    "v_mfma_f32_16x16x32_bf16 v[244:247], v[60:63], a[196:199], v[244:247]\n\t"
    "v_mfma_f32_16x16x32_bf16 v[248:251], v[60:63], a[200:203], v[248:251]\n\t"
    "v_mfma_f32_16x16x32_bf16 v[252:255], v[60:63], a[204:207], v[252:255]\n\t"
    "s_waitcnt vmcnt(24)\n\t"
    "s_waitcnt lgkmcnt(0)\n\t"
    "s_barrier\n\t"
    "v_mfma_f32_16x16x32_bf16 v[128:131], v[64:67], a[208:211], v[128:131]\n\t"
    "ds_read_b128 v[32:35], v5 offset:49152\n\t"
    "v_mfma_f32_16x16x32_bf16 v[132:135], v[64:67], a[212:215], v[132:135]\n\t"
    "v_mfma_f32_16x16x32_bf16 v[136:139], v[64:67], a[216:219], v[136:139]\n\t"
    "ds_read_b128 v[36:39], v5 offset:51200\n\t"
    "v_mfma_f32_16x16x32_bf16 v[140:143], v[64:67], a[220:223], v[140:143]\n\t"
    "s_add_u32 s46, s41, 768\n\t"
    "s_add_u32 m0, %[mb], 98304\n\t"
    "s_nop 0\n\t"
    "buffer_load_dwordx4 v0, %[rx], s46 offen lds\n\t"
    "v_mfma_f32_16x16x32_bf16 v[144:147], v[68:71], a[208:211], v[144:147]\n\t"
    "ds_read_b128 v[40:43], v5 offset:53248\n\t"
    "v_mfma_f32_16x16x32_bf16 v[148:151], v[68:71], a[212:215], v[148:151]\n\t"
    "v_mfma_f32_16x16x32_bf16 v[152:155], v[68:71], a[216:219], v[152:155]\n\t"
    "ds_read_b128 v[44:47], v5 offset:55296\n\t"
    "v_mfma_f32_16x16x32_bf16 v[156:159], v[68:71], a[220:223], v[156:159]\n\t"
    "v_mfma_f32_16x16x32_bf16 v[160:163], v[72:75], a[208:211], v[160:163]\n\t"
    "ds_read_b128 v[48:51], v5 offset:57344\n\t"
    "v_mfma_f32_16x16x32_bf16 v[164:167], v[72:75], a[212:215], v[164:167]\n\t"
    "s_add_u32 m0, %[mb], 99328\n\t"
    "s_nop 0\n\t"
    "buffer_load_dwordx4 v1, %[rx], s46 offen lds\n\t"
    "v_mfma_f32_16x16x32_bf16 v[168:171], v[72:75], a[216:219], v[168:171]\n\t"
    "ds_read_b128 v[52:55], v5 offset:59392\n\t"
    "v_mfma_f32_16x16x32_bf16 v[172:175], v[72:75], a[220:223], v[172:175]\n\t"
    "v_mfma_f32_16x16x32_bf16 v[176:179], v[76:79], a[208:211], v[176:179]\n\t"
    "ds_read_b128 v[56:59], v5 offset:61440\n\t"
    "v_mfma_f32_16x16x32_bf16 v[180:183], v[76:79], a[212:215], v[180:183]\n\t"
    "v_mfma_f32_16x16x32_bf16 v[184:187], v[76:79], a[216:219], v[184:187]\n\t"
    "ds_read_b128 v[60:63], v5 offset:63488\n\t"
    "v_mfma_f32_16x16x32_bf16 v[188:191], v[76:79], a[220:223], v[188:191]\n\t"
    "s_add_u32 m0, %[mb], 100352\n\t"
    "s_nop 0\n\t"
    "buffer_load_dwordx4 v2, %[rx], s46 offen lds\n\t"
    "v_mfma_f32_16x16x32_bf16 v[192:195], v[80:83], a[208:211], v[192:195]\n\t"
    "v_mfma_f32_16x16x32_bf16 v[196:199], v[80:83], a[212:215], v[196:199]\n\t"
    "v_mfma_f32_16x16x32_bf16 v[200:203], v[80:83], a[216:219], v[200:203]\n\t"
    "v_mfma_f32_16x16x32_bf16 v[204:207], v[80:83], a[220:223], v[204:207]\n\t"
    "v_mfma_f32_16x16x32_bf16 v[208:211], v[84:87], a[208:211], v[208:211]\n\t"
    "v_mfma_f32_16x16x32_bf16 v[212:215], v[84:87], a[212:215], v[212:215]\n\t"
    "s_add_u32 m0, %[mb], 101376\n\t"
    "s_nop 0\n\t"
    "buffer_load_dwordx4 v3, %[rx], s46 offen lds\n\t"
    "v_mfma_f32_16x16x32_bf16 v[216:219], v[84:87], a[216:219], v[216:219]\n\t"
    "v_mfma_f32_16x16x32_bf16 v[220:223], v[84:87], a[220:223], v[220:223]\n\t"
    "v_mfma_f32_16x16x32_bf16 v[224:227], v[88:91], a[208:211], v[224:227]\n\t"
    "v_mfma_f32_16x16x32_bf16 v[228:231], v[88:91], a[212:215], v[228:231]\n\t"
    "v_mfma_f32_16x16x32_bf16 v[232:235], v[88:91], a[216:219], v[232:235]\n\t"
    "v_mfma_f32_16x16x32_bf16 v[236:239], v[88:91], a[220:223], v[236:239]\n\t"
    "v_mfma_f32_16x16x32_bf16 v[240:243], v[92:95], a[208:211], v[240:243]\n\t"
    "v_mfma_f32_16x16x32_bf16 v[244:247], v[92:95], a[212:215], v[244:247]\n\t"
    "v_mfma_f32_16x16x32_bf16 v[248:251], v[92:95], a[216:219], v[248:251]\n\t"
    "v_mfma_f32_16x16x32_bf16 v[252:255], v[92:95], a[220:223], v[252:255]\n\t"
    "s_waitcnt lgkmcnt(7)\n\t"
    "v_mfma_f32_16x16x32_bf16 v[128:131], v[32:35], a[224:227], v[128:131]\n\t"
    "ds_read_b128 v[64:67], v7 offset:49152\n\t"
    "v_mfma_f32_16x16x32_bf16 v[132:135], v[32:35], a[228:231], v[132:135]\n\t"
    "v_mfma_f32_16x16x32_bf16 v[136:139], v[32:35], a[232:235], v[136:139]\n\t"
    "ds_read_b128 v[68:71], v7 offset:51200\n\t"
    "v_mfma_f32_16x16x32_bf16 v[140:143], v[32:35], a[236:239], v[140:143]\n\t"
    "s_waitcnt lgkmcnt(8)\n\t"
    "v_mfma_f32_16x16x32_bf16 v[144:147], v[36:39], a[224:227], v[144:147]\n\t"
    "ds_read_b128 v[72:75], v7 offset:53248\n\t"
    "v_mfma_f32_16x16x32_bf16 v[148:151], v[36:39], a[228:231], v[148:151]\n\t"
    "v_mfma_f32_16x16x32_bf16 v[152:155], v[36:39], a[232:235], v[152:155]\n\t"
    "ds_read_b128 v[76:79], v7 offset:55296\n\t"
    "v_mfma_f32_16x16x32_bf16 v[156:159], v[36:39], a[236:239], v[156:159]\n\t"
    "s_waitcnt lgkmcnt(9)\n\t"
    "v_mfma_f32_16x16x32_bf16 v[160:163], v[40:43], a[224:227], v[160:163]\n\t"
    "ds_read_b128 v[80:83], v7 offset:57344\n\t"
    "v_mfma_f32_16x16x32_bf16 v[164:167], v[40:43], a[228:231], v[164:167]\n\t"
    "v_mfma_f32_16x16x32_bf16 v[168:171], v[40:43], a[232:235], v[168:171]\n\t"
    "ds_read_b128 v[84:87], v7 offset:59392\n\t"
    "v_mfma_f32_16x16x32_bf16 v[172:175], v[40:43], a[236:239], v[172:175]\n\t"
    "s_waitcnt lgkmcnt(10)\n\t"
    "v_mfma_f32_16x16x32_bf16 v[176:179], v[44:47], a[224:227], v[176:179]\n\t"
    "ds_read_b128 v[88:91], v7 offset:61440\n\t"
    "v_mfma_f32_16x16x32_bf16 v[180:183], v[44:47], a[228:231], v[180:183]\n\t"
    "v_mfma_f32_16x16x32_bf16 v[184:187], v[44:47], a[232:235], v[184:187]\n\t"
    "ds_read_b128 v[92:95], v7 offset:63488\n\t"
    "v_mfma_f32_16x16x32_bf16 v[188:191], v[44:47], a[236:239], v[188:191]\n\t"
    "s_waitcnt lgkmcnt(11)\n\t"
    "v_mfma_f32_16x16x32_bf16 v[192:195], v[48:51], a[224:227], v[192:195]\n\t"
    "v_mfma_f32_16x16x32_bf16 v[196:199], v[48:51], a[228:231], v[196:199]\n\t"
    "v_mfma_f32_16x16x32_bf16 v[200:203], v[48:51], a[232:235], v[200:203]\n\t"
    "v_mfma_f32_16x16x32_bf16 v[204:207], v[48:51], a[236:239], v[204:207]\n\t"
    "s_waitcnt lgkmcnt(10)\n\t"
    "v_mfma_f32_16x16x32_bf16 v[208:211], v[52:55], a[224:227], v[208:211]\n\t"
    "v_mfma_f32_16x16x32_bf16 v[212:215], v[52:55], a[228:231], v[212:215]\n\t"
    "v_mfma_f32_16x16x32_bf16 v[216:219], v[52:55], a[232:235], v[216:219]\n\t"
    "v_mfma_f32_16x16x32_bf16 v[220:223], v[52:55], a[236:239], v[220:223]\n\t"
    "s_waitcnt lgkmcnt(9)\n\t"
    "v_mfma_f32_16x16x32_bf16 v[224:227], v[56:59], a[224:227], v[224:227]\n\t"
    "v_mfma_f32_16x16x32_bf16 v[228:231], v[56:59], a[228:231], v[228:231]\n\t"
    "v_mfma_f32_16x16x32_bf16 v[232:235], v[56:59], a[232:235], v[232:235]\n\t"
    "v_mfma_f32_16x16x32_bf16 v[236:239], v[56:59], a[236:239], v[236:239]\n\t"
    "s_waitcnt lgkmcnt(8)\n\t"
    "v_mfma_f32_16x16x32_bf16 v[240:243], v[60:63], a[224:227], v[240:243]\n\t"
    "v_mfma_f32_16x16x32_bf16 v[244:247], v[60:63], a[228:231], v[244:247]\n\t"
    "v_mfma_f32_16x16x32_bf16 v[248:251], v[60:63], a[232:235], v[248:251]\n\t"
    "v_mfma_f32_16x16x32_bf16 v[252:255], v[60:63], a[236:239], v[252:255]\n\t"
    "s_waitcnt vmcnt(24)\n\t"
    "s_waitcnt lgkmcnt(0)\n\t"
    "s_barrier\n\t"
    "v_mfma_f32_16x16x32_bf16 v[128:131], v[64:67], a[240:243], v[128:131]\n\t"
    "ds_read_b128 v[32:35], v4 offset:0\n\t"
    "v_mfma_f32_16x16x32_bf16 v[132:135], v[64:67], a[244:247], v[132:135]\n\t"
    "v_mfma_f32_16x16x32_bf16 v[136:139], v[64:67], a[248:251], v[136:139]\n\t"
    "ds_read_b128 v[36:39], v4 offset:2048\n\t"
    "v_mfma_f32_16x16x32_bf16 v[140:143], v[64:67], a[252:255], v[140:143]\n\t"
    "s_add_u32 s46, s41, 896\n\t"
    "s_add_u32 m0, %[mb], 114688\n\t"
    "s_nop 0\n\t"
    "buffer_load_dwordx4 v0, %[rx], s46 offen lds\n\t"
    "v_mfma_f32_16x16x32_bf16 v[144:147], v[68:71], a[240:243], v[144:147]\n\t"
    "ds_read_b128 v[40:43], v4 offset:4096\n\t"
    "v_mfma_f32_16x16x32_bf16 v[148:151], v[68:71], a[244:247], v[148:151]\n\t"
    "v_mfma_f32_16x16x32_bf16 v[152:155], v[68:71], a[248:251], v[152:155]\n\t"
    "ds_read_b128 v[44:47], v4 offset:6144\n\t"
    "v_mfma_f32_16x16x32_bf16 v[156:159], v[68:71], a[252:255], v[156:159]\n\t"
    "v_mfma_f32_16x16x32_bf16 v[160:163], v[72:75], a[240:243], v[160:163]\n\t"
    "ds_read_b128 v[48:51], v4 offset:8192\n\t"
    "v_mfma_f32_16x16x32_bf16 v[164:167], v[72:75], a[244:247], v[164:167]\n\t"
    "s_add_u32 m0, %[mb], 115712\n\t"
    "s_nop 0\n\t"
    "buffer_load_dwordx4 v1, %[rx], s46 offen lds\n\t"
    "v_mfma_f32_16x16x32_bf16 v[168:171], v[72:75], a[248:251], v[168:171]\n\t"
    "ds_read_b128 v[52:55], v4 offset:10240\n\t"
    "v_mfma_f32_16x16x32_bf16 v[172:175], v[72:75], a[252:255], v[172:175]\n\t"
    "v_mfma_f32_16x16x32_bf16 v[176:179], v[76:79], a[240:243], v[176:179]\n\t"
    "ds_read_b128 v[56:59], v4 offset:12288\n\t"
    "v_mfma_f32_16x16x32_bf16 v[180:183], v[76:79], a[244:247], v[180:183]\n\t"
    "v_mfma_f32_16x16x32_bf16 v[184:187], v[76:79], a[248:251], v[184:187]\n\t"
    "ds_read_b128 v[60:63], v4 offset:14336\n\t"
    "v_mfma_f32_16x16x32_bf16 v[188:191], v[76:79], a[252:255], v[188:191]\n\t"
    "s_add_u32 m0, %[mb], 116736\n\t"
    "s_nop 0\n\t"
    "buffer_load_dwordx4 v2, %[rx], s46 offen lds\n\t"
    "v_mfma_f32_16x16x32_bf16 v[192:195], v[80:83], a[240:243], v[192:195]\n\t"
    "v_mfma_f32_16x16x32_bf16 v[196:199], v[80:83], a[244:247], v[196:199]\n\t"
    "v_mfma_f32_16x16x32_bf16 v[200:203], v[80:83], a[248:251], v[200:203]\n\t"
    "v_mfma_f32_16x16x32_bf16 v[204:207], v[80:83], a[252:255], v[204:207]\n\t"
    "v_mfma_f32_16x16x32_bf16 v[208:211], v[84:87], a[240:243], v[208:211]\n\t"
    "v_mfma_f32_16x16x32_bf16 v[212:215], v[84:87], a[244:247], v[212:215]\n\t"
    "s_add_u32 m0, %[mb], 117760\n\t"
    "s_nop 0\n\t"
    "buffer_load_dwordx4 v3, %[rx], s46 offen lds\n\t"
    "v_mfma_f32_16x16x32_bf16 v[216:219], v[84:87], a[248:251], v[216:219]\n\t"
    "v_mfma_f32_16x16x32_bf16 v[220:223], v[84:87], a[252:255], v[220:223]\n\t"
    "v_mfma_f32_16x16x32_bf16 v[224:227], v[88:91], a[240:243], v[224:227]\n\t"
    "v_mfma_f32_16x16x32_bf16 v[228:231], v[88:91], a[244:247], v[228:231]\n\t"
    "v_mfma_f32_16x16x32_bf16 v[232:235], v[88:91], a[248:251], v[232:235]\n\t"
    "v_mfma_f32_16x16x32_bf16 v[236:239], v[88:91], a[252:255], v[236:239]\n\t"
    "v_mfma_f32_16x16x32_bf16 v[240:243], v[92:95], a[240:243], v[240:243]\n\t"
    "v_mfma_f32_16x16x32_bf16 v[244:247], v[92:95], a[244:247], v[244:247]\n\t"
    "v_mfma_f32_16x16x32_bf16 v[248:251], v[92:95], a[248:251], v[248:251]\n\t"
    "v_mfma_f32_16x16x32_bf16 v[252:255], v[92:95], a[252:255], v[252:255]\n\t"
    "v_cvt_pk_bf16_f32 v96, v128, v129\n\t"
    "v_cvt_pk_bf16_f32 v97, v130, v131\n\t"
    "v_cvt_pk_bf16_f32 v98, v144, v145\n\t"
    "v_cvt_pk_bf16_f32 v99, v146, v147\n\t"
    "buffer_store_dwordx4 v[96:99], v8, %[rd], s43 offen offset:0 sc1\n\t"
    "v_cvt_pk_bf16_f32 v100, v132, v133\n\t"
    "v_cvt_pk_bf16_f32 v101, v134, v135\n\t"
    "v_cvt_pk_bf16_f32 v102, v148, v149\n\t"
    "v_cvt_pk_bf16_f32 v103, v150, v151\n\t"
    "buffer_store_dwordx4 v[100:103], v8, %[rd], s43 offen offset:1024 sc1\n\t"
    "v_cvt_pk_bf16_f32 v104, v136, v137\n\t"
    "v_cvt_pk_bf16_f32 v105, v138, v139\n\t"
    "v_cvt_pk_bf16_f32 v106, v152, v153\n\t"
    "v_cvt_pk_bf16_f32 v107, v154, v155\n\t"
    "buffer_store_dwordx4 v[104:107], v8, %[rd], s43 offen offset:2048 sc1\n\t"
    "v_cvt_pk_bf16_f32 v108, v140, v141\n\t"
    "v_cvt_pk_bf16_f32 v109, v142, v143\n\t"
    "v_cvt_pk_bf16_f32 v110, v156, v157\n\t"
    "v_cvt_pk_bf16_f32 v111, v158, v159\n\t"
    "buffer_store_dwordx4 v[108:111], v8, %[rd], s43 offen offset:3072 sc1\n\t"
    "s_add_u32 s45, s43, %[s2]\n\t"
    "v_cvt_pk_bf16_f32 v96, v160, v161\n\t"
    "v_cvt_pk_bf16_f32 v97, v162, v163\n\t"
    "v_cvt_pk_bf16_f32 v98, v176, v177\n\t"
    "v_cvt_pk_bf16_f32 v99, v178, v179\n\t"
    "buffer_store_dwordx4 v[96:99], v8, %[rd], s45 offen offset:0 sc1\n\t"
    "v_cvt_pk_bf16_f32 v100, v164, v165\n\t"
    "v_cvt_pk_bf16_f32 v101, v166, v167\n\t"
    "v_cvt_pk_bf16_f32 v102, v180, v181\n\t"
    "v_cvt_pk_bf16_f32 v103, v182, v183\n\t"
    "buffer_store_dwordx4 v[100:103], v8, %[rd], s45 offen offset:1024 sc1\n\t"
    "v_cvt_pk_bf16_f32 v104, v168, v169\n\t"
    "v_cvt_pk_bf16_f32 v105, v170, v171\n\t"
    "v_cvt_pk_bf16_f32 v106, v184, v185\n\t"
    "v_cvt_pk_bf16_f32 v107, v186, v187\n\t"
    "buffer_store_dwordx4 v[104:107], v8, %[rd], s45 offen offset:2048 sc1\n\t"
    "v_cvt_pk_bf16_f32 v108, v172, v173\n\t"
    "v_cvt_pk_bf16_f32 v109, v174, v175\n\t"
    "v_cvt_pk_bf16_f32 v110, v188, v189\n\t"
    "v_cvt_pk_bf16_f32 v111, v190, v191\n\t"
    "buffer_store_dwordx4 v[108:111], v8, %[rd], s45 offen offset:3072 sc1\n\t"
    "s_add_u32 s45, s45, %[s2]\n\t"
    "v_cvt_pk_bf16_f32 v96, v192, v193\n\t"
    "v_cvt_pk_bf16_f32 v97, v194, v195\n\t"
    "v_cvt_pk_bf16_f32 v98, v208, v209\n\t"
    "v_cvt_pk_bf16_f32 v99, v210, v211\n\t"
    "buffer_store_dwordx4 v[96:99], v8, %[rd], s45 offen offset:0 sc1\n\t"
    "v_cvt_pk_bf16_f32 v100, v196, v197\n\t"
    "v_cvt_pk_bf16_f32 v101, v198, v199\n\t"
    "v_cvt_pk_bf16_f32 v102, v212, v213\n\t"
    "v_cvt_pk_bf16_f32 v103, v214, v215\n\t"
    "buffer_store_dwordx4 v[100:103], v8, %[rd], s45 offen offset:1024 sc1\n\t"
    "v_cvt_pk_bf16_f32 v104, v200, v201\n\t"
    "v_cvt_pk_bf16_f32 v105, v202, v203\n\t"
    "v_cvt_pk_bf16_f32 v106, v216, v217\n\t"
    "v_cvt_pk_bf16_f32 v107, v218, v219\n\t"
    "buffer_store_dwordx4 v[104:107], v8, %[rd], s45 offen offset:2048 sc1\n\t"
    "v_cvt_pk_bf16_f32 v108, v204, v205\n\t"
    "v_cvt_pk_bf16_f32 v109, v206, v207\n\t"
    "v_cvt_pk_bf16_f32 v110, v220, v221\n\t"
    "v_cvt_pk_bf16_f32 v111, v222, v223\n\t"
    "buffer_store_dwordx4 v[108:111], v8, %[rd], s45 offen offset:3072 sc1\n\t"
    "s_add_u32 s45, s45, %[s2]\n\t"
    "v_cvt_pk_bf16_f32 v96, v224, v225\n\t"
    "v_cvt_pk_bf16_f32 v97, v226, v227\n\t"
    "v_cvt_pk_bf16_f32 v98, v240, v241\n\t"
    "v_cvt_pk_bf16_f32 v99, v242, v243\n\t"
    "buffer_store_dwordx4 v[96:99], v8, %[rd], s45 offen offset:0 sc1\n\t"
    "v_cvt_pk_bf16_f32 v100, v228, v229\n\t"
    "v_cvt_pk_bf16_f32 v101, v230, v231\n\t"
    "v_cvt_pk_bf16_f32 v102, v244, v245\n\t"
    "v_cvt_pk_bf16_f32 v103, v246, v247\n\t"
    "buffer_store_dwordx4 v[100:103], v8, %[rd], s45 offen offset:1024 sc1\n\t"
    "v_cvt_pk_bf16_f32 v104, v232, v233\n\t"
    "v_cvt_pk_bf16_f32 v105, v234, v235\n\t"
    "v_cvt_pk_bf16_f32 v106, v248, v249\n\t"
    "v_cvt_pk_bf16_f32 v107, v250, v251\n\t"
    "buffer_store_dwordx4 v[104:107], v8, %[rd], s45 offen offset:2048 sc1\n\t"
    "v_cvt_pk_bf16_f32 v108, v236, v237\n\t"
    "v_cvt_pk_bf16_f32 v109, v238, v239\n\t"
    "v_cvt_pk_bf16_f32 v110, v252, v253\n\t"
    "v_cvt_pk_bf16_f32 v111, v254, v255\n\t"
    "buffer_store_dwordx4 v[108:111], v8, %[rd], s45 offen offset:3072 sc1\n\t"
    "s_mov_b32 s44, s47\n\t"
    "s_add_u32 s47, s47, %[fs]\n\t"
    "s_add_u32 s43, s43, %[dt]\n\t"
    "s_sub_u32 s40, s40, 1\n\t"
    "s_cmp_lg_u32 s40, 0\n\t"
    "s_cbranch_scc1 1b\n\t"
    "s_waitcnt vmcnt(0) lgkmcnt(0)\n\t"
    "s_barrier\n\t"
    "s_cmp_lg_u32 %[w], 0\n\t"
    "s_cbranch_scc1 2f\n\t"
    "s_mov_b64 exec, 1\n\t"
    "buffer_store_dword v9, off, %[rc], s44 sc1\n\t"
    "s_mov_b64 exec, -1\n\t"
    "2:\n\t"
    "s_mov_b32 m0, s42\n\t"
    "9:\n\t"
    GEMM_OPERANDS);
}

constexpr int CH_KX = 131072, CH_OI = CH_KX + 8192, CH_FI = CH_OI + 8192, CH_LDS = CH_FI + 4096;

__device__ __forceinline__ bool nosent(v4u x) { return x.x != SENT && x.y != SENT && x.z != SENT && x.w != SENT; }

__device__ __forceinline__ int wsl_swz(int r) { return ((r >> 1) & 1) | (((r >> 4) & 3) << 1); }
__device__ __forceinline__ void wsl_issue(const Ctx& p, u32 buf, const u16* mat, int h, int hf) {
  const int lane = lane_id(), w = p.w, g = lane >> 4, n = lane & 15;
  const __amdgpu_buffer_rsrc_t rs = srd(mat, (size_t)DM * HD * 2);
  const u32 vo = g * (HD * 2) + ((n ^ (g & 2)) << 4), so = (2 * w + hf) * 64 * (HD * 2) + h * DH * 2, k = m0_save();
  static_for<16>([&](auto I) { constexpr int i = I.value; blds16<i * 1024, (i >> 2) << 6, i * 4 * HD * 2>(rs, vo, so, buf + w * 16384); });
  m0_restore(k);
}
template <int HF>
__device__ __forceinline__ void wsl_read(const Ctx& p, v32u (&dst)[4], u32 buf) {
  const int lane = lane_id(), w = p.w, g = lane >> 4, n = lane & 15;
#pragma unroll
  for (int q = 0; q < 4; ++q) {
    const int r = 8 * (q >> 1) + 2 * g + (q & 1) + 16 * (n >> 2);
#pragma unroll
    for (int t = 0; t < 2; ++t) {
      const u32 a = r * 256 + (((2 * w + t) ^ wsl_swz(r)) << 5) + (n & 3) * 8;
#pragma unroll
      for (int i = 0; i < 4; ++i) {
        const int ks = 2 * i + HF;
        const v2u v = trread(buf + a + ((i ^ w) << 14));
        dst[2 * t + (ks >> 2)][(ks & 3) * 8 + 2 * q] = v.x;
        dst[2 * t + (ks >> 2)][(ks & 3) * 8 + 2 * q + 1] = v.y;
      }
    }
  }
}
__device__ __forceinline__ int wo_swz(int r) { return (r & 1) | (((r >> 2) & 3) << 1); }
__device__ __forceinline__ void wo_issue(const Ctx& p, u32 buf, int h, int hc) {
  const __amdgpu_buffer_rsrc_t rs = srd(p.wo, (size_t)HD * DM * 2);
  const int lane = lane_id();
  const u32 vo = (lane >> 5) * (DM * 2) + (((lane & 31) ^ ((lane >> 5) << 1)) << 4), so = (h * DH + p.w * 32) * (DM * 2) + hc * 512, k = m0_save();
  static_for<16>([&](auto I) { constexpr int i = I.value; blds16<i * 1024, ((i >> 1) & 3) << 6, i * 2 * DM * 2>(rs, vo, so, buf + p.w * 16384); });
  m0_restore(k);
}
__device__ __forceinline__ void wo_relay(const Ctx& p, u32 buf) {
  const int lane = lane_id(), w = p.w, g = lane >> 4, n = lane & 15;
  const int qq = n >> 2, r = 16 * (g >> 1) + 4 * (g & 1) + (qq & 1) + 8 * (qq >> 1);
  const u32 a0 = r * 512 + (((w * 4) ^ wo_swz(r)) << 5) + (n & 3) * 8;
  v2u tmp[8][4];
#pragma unroll
  for (int i = 0; i < 8; ++i)
#pragma unroll
    for (int q = 0; q < 4; ++q) tmp[i][q] = trread(buf + (a0 ^ (u32)((i >> 1) << 5)) + (i & 1) * 32768 + (q >> 1) * 16384 + (q & 1) * 1024);
  WAIT_LGKM0();
  BARRIER();
#pragma unroll
  for (int i = 0; i < 8; ++i)
#pragma unroll
    for (int hf = 0; hf < 2; ++hf)
      *(LAS v4u*)(buf + w * 16384 + i * 2048 + hf * 1024 + lane * 16) = v4u{tmp[i][2 * hf].x, tmp[i][2 * hf].y, tmp[i][2 * hf + 1].x, tmp[i][2 * hf + 1].y};
}
#define LANDED() do { WAIT_VM(16); BARRIER(); if (p.b == 0) { TLS(4400 + tlc); ++tlc; } } while (0)
#define CONSUMED() do { WAIT_LGKM0(); BARRIER(); } while (0)

#define UNI(x) __builtin_amdgcn_readfirstlane(x)

__device__ __forceinline__ void chain_role(Ctx& p, u32 lds, int c) {
  const int h = c >> 1, hc = c & 1;
  const int lane = lane_id(), w = p.w, g = lane >> 4, n = lane & 15;
  u32* ctrl = (u32*)(p.ws + WS_CTRL);
  const __amdgpu_buffer_rsrc_t rw = __builtin_amdgcn_make_buffer_rsrc((void*)(p.ws + WS_WIN), 0, (int)(2 * MB), RSRC_W3);
  const __amdgpu_buffer_rsrc_t rk = __builtin_amdgcn_make_buffer_rsrc((void*)(p.ws + WS_AB + (size_t)h * NSEG * AB_BLK), 0, NSEG * AB_BLK, RSRC_W3);
  v32u wq[4], wk[4], wv[4];
  int tlc = 0;
  u32 ev = epoch_ask(p);
  const u32 bA = lds, bB = lds + 65536;
  wsl_issue(p, bA, p.wq, h, 0);
  wsl_issue(p, bB, p.wq, h, 1);
  TELL_XCD(ev, 32);
  if (w < 2) asm volatile("buffer_load_dword v255, %0, %1, 0 offen sc1" :: "v"(lane * 32768 + 24576), "s"(w == 0 ? rk : rw) : "memory", "v255");
  LANDED(); wsl_read<0>(p, wq, bA); CONSUMED(); wsl_issue(p, bA, p.wk, h, 0);
  LANDED(); wsl_read<1>(p, wq, bB); CONSUMED(); wsl_issue(p, bB, p.wk, h, 1);
  asm volatile("" : "+{a[0:31]}"(wq[0]), "+{a[32:63]}"(wq[1]), "+{a[64:95]}"(wq[2]), "+{a[96:127]}"(wq[3]));
  LANDED(); wsl_read<0>(p, wk, bA); CONSUMED(); wsl_issue(p, bA, p.wv, h, 0);
  LANDED(); wsl_read<1>(p, wk, bB); CONSUMED(); wsl_issue(p, bB, p.wv, h, 1);
  asm volatile("" : "+{a[128:159]}"(wk[0]), "+{a[160:191]}"(wk[1]), "+{a[192:223]}"(wk[2]), "+{a[224:255]}"(wk[3]));
  LANDED(); wsl_read<0>(p, wv, bA); CONSUMED(); wo_issue(p, bA, h, hc);
  LANDED(); wsl_read<1>(p, wv, bB);
  asm volatile("" : "+{v[128:159]}"(wv[0]), "+{v[160:191]}"(wv[1]), "+{v[192:223]}"(wv[2]), "+{v[224:255]}"(wv[3]));
  WAIT_VM(0); BARRIER();
  const u32 coff = lane < 32 ? CT_INIT + 4 * lane : lane < 34 ? CT_OFF + (p.b & 15) * 128 + 4 * (lane - 32) : lane == 34 ? CT_PAR + 4 : CT_KV + h * 128;
  u32 cw;
  asm volatile("global_load_dword %0, %1, %2 sc1" : "=v"(cw) : "v"(coff), "s"(p.ws + WS_CTRL) : "memory");
  wo_relay(p, bA);
  asm volatile("s_waitcnt vmcnt(0)" : "+v"(cw), "+v"(ev) :: "memory");
  if (p.b == 0) TLS(4410);
  p.tag = UNI(ev) + 1u;
  bool checked = p.lab >= 8;
  for (u32 spins = 0; spins < SPIN_CAP; ++spins) {
    const u32 d = __builtin_amdgcn_readlane(cw, 33) - (p.tag ^ TAG2);
    if (!checked && __builtin_amdgcn_readlane(cw, 32) == p.tag && d < 8u) {
      if (((p.b + d) & 7) != (u32)p.lab) restart(p, (p.b + d) & 7);
      checked = true;
    }
    if (checked && __all(lane == 34 ? cw - (p.tag ^ TAG2) < 2u : (cw == p.tag || lane == 32 || lane == 33))) break;
    cw = __hip_atomic_load(ctw(p, coff), RLX_AGENT);
  }
  if (w == 0 && lane == 0) __hip_atomic_store(ctw(p, CT_ARR + p.b * 4), p.tag, RLX_AGENT);
  const int par = (__builtin_amdgcn_readlane(cw, 34) - (p.tag ^ TAG2)) & 1;
  v32u cv;
  cv[0] = remap(WIN_MEM + (h >> 3) * 8192 + w * 2048 + lane * 16, par);
  cv[1] = remap(WIN_PART + (2 * c + (w & 1)) * 2048 + lane * 16, par);
  cv[2] = remap(WIN_PART + (((4 * hc + w) * 4 + (n >> 2)) * 4 + g) * 2048 + h * 64 + (n & 3) * 16, par);
  cv[3] = w >= 2 ? 0x40000000 : remap(WIN_MEM + (((n >> 1) & 1) + 2 * (n >> 3)) * 8192 + (2 * c + w) * 64 + (2 * (n & 1) + ((n >> 2) & 1)) * 16 + g * 4, par);
  cv[4] = w * 2048 + lane * 16;
  cv[5] = (n & 1) ? 0x3F800000u : 0x00003F80u;
  cv[6] = lds + CH_FI + (32 * w + n) * 32 + g * 8;
  cv[7] = lds + CH_FI + (4 * g + (n >> 2)) * 32 + (n & 3) * 8;
  cv[8] = lds + CH_OI + (((n & 2) ? 0 : 8) + 2 * (n >> 2) + (n & 1)) * 272 + (32 * w + 4 * g) * 2;
  cv[9] = lds + CH_OI + (n >> 1) * 272 + (16 * g + 8 * (n & 1)) * 2;
  cv[10] = lds + w * 16384 + lane * 16;
  cv[11] = 0;
  cv[12] = ((lane & 16) ? AB_V : AB_K) + w * 2048 + n * 128;
  cv[13] = lds + CH_KX + lane * 16;
  cv[14] = (n & 1) ? 0xEEEE : 0x4444;
#pragma unroll
  for (int i = 15; i < 32; ++i) cv[i] = (i >= 20 && i < 24) ? 0x3F803F80u : 0u;
  const __amdgpu_buffer_rsrc_t rc = __builtin_amdgcn_make_buffer_rsrc((void*)(p.ws + WS_CTRL), 0, CT_BYTES, RSRC_W3);
  TLS(p.b * 8 + 1);
  TLS(p.b * 8 + 2);
  WAIT_LGKM0();
  BARRIER();
  asm volatile(
    "s_mov_b32 s40, 0\n\t"
    "s_mov_b32 s41, 0\n\t"
    "s_mov_b32 s42, 0\n\t"
    "s_mov_b32 s43, %[tg]\n\t"
    "s_mov_b32 s44, 0x2000\n\t"
    "s_mov_b32 s45, 0x40000000\n\t"
    "s_mov_b32 s46, 0x20000\n\t"
    "s_mov_b32 s51, 0\n\t"
    "s_mov_b32 s52, 1\n\t"
    "s_mov_b32 s47, 0x40000000\n\t"
    "s_mov_b32 s48, 0x7fffffff\n\t"
    "s_mov_b32 s49, 0x3e0293ee\n\t"
    "s_mov_b32 s50, 0x8000\n\t"
    "s_mov_b32 s53, 0x10000\n\t"
    "s_mov_b32 s58, s49\n\t"
    "s_add_u32 s55, %[kf], 4\n\t"
    ".p2align 6\n\t"
    "s_nop 0\n\t"
    "s_nop 0\n\t"
    "s_nop 0\n\t"
    "10:\n\t"
    "buffer_load_dwordx4 v[72:75], v4, %[rk], s42 offen offset:0 sc1\n\t"
    "buffer_load_dwordx4 v[76:79], v4, %[rk], s42 offen offset:1024 sc1\n\t"
    "buffer_load_dwordx4 v[32:35], v4, %[rk], s44 offen offset:0 sc1\n\t"
    "buffer_load_dwordx4 v[36:39], v4, %[rk], s44 offen offset:1024 sc1\n\t"
    "v_mov_b64 v[48:49], 0\n\t"
    "v_mov_b64 v[50:51], 0\n\t"
    "v_mov_b64 v[52:53], 0\n\t"
    "v_mov_b64 v[54:55], 0\n\t"
    "v_mov_b64 v[56:57], 0\n\t"
    "v_mov_b64 v[58:59], 0\n\t"
    "v_mov_b64 v[60:61], 0\n\t"
    "v_mov_b64 v[62:63], 0\n\t"
    "v_mov_b64 v[64:65], 0\n\t"
    "v_mov_b64 v[66:67], 0\n\t"
    "v_mov_b64 v[68:69], 0\n\t"
    "v_mov_b64 v[70:71], 0\n\t"
    "v_add_u32 v43, %[w2k], v13\n\t"
    "v_mov_b32 v28, s48\n\t"
    "s_lshl_b32 s54, s40, 5\n\t"
    "s_add_u32 s54, s54, %[pf]\n\t"
    "s_cmp_lt_u32 s40, 2\n\t"
    "s_cselect_b32 s54, 0x40000000, s54\n\t"
    "v_mov_b32 v30, %[t]\n\t"
    "v_mov_b32 v31, %[t2]\n\t"
    "v_xor_b32 v44, 0x800, v43\n\t"
    "v_xor_b32 v45, 0x1000, v43\n\t"
    "v_xor_b32 v46, 0x1800, v43\n\t"
    "v_min_i32 v16, 0x7ffffffe, v16\n\t"
    "v_min_i32 v17, 0x7ffffffe, v17\n\t"
    "v_min_i32 v18, 0x7ffffffe, v18\n\t"
    "v_min_i32 v19, 0x7ffffffe, v19\n\t"
    "s_nop 15\n\t"
    "buffer_load_dwordx4 v[96:99], v0, %[rw], s41 offen offset:0 sc1\n\t"
    "buffer_load_dwordx4 v[100:103], v0, %[rw], s41 offen offset:1024 sc1\n\t"
    "s_waitcnt vmcnt(4)\n\t"
    "ds_write_b128 v43, v[72:75] offset:0\n\t"
    "ds_write_b128 v43, v[76:79] offset:1024\n\t"
    ".p2align 5\n\t"
    "62:\n\t"
    "s_waitcnt vmcnt(0)\n\t"
    "ds_write_b128 v43, v[96:99] offset:8192\n\t"
    "ds_write_b128 v43, v[100:103] offset:9216\n\t"
    "v_smfmac_f32_16x16x64_bf16 v[48:51], v[96:99], a[0:7], v14\n\t"
    "v_max3_i32 v40, v96, v97, v98\n\t"
    "v_smfmac_f32_16x16x64_bf16 v[56:59], v[96:99], a[128:135], v14\n\t"
    "v_max3_i32 v41, v99, v100, v101\n\t"
    "v_smfmac_f32_16x16x64_bf16 v[52:55], v[96:99], a[64:71], v14\n\t"
    "v_max3_i32 v40, v40, v102, v103\n\t"
    "v_max_i32 v40, v40, v41\n\t"
    "v_smfmac_f32_16x16x64_bf16 v[60:63], v[96:99], a[192:199], v14\n\t"
    "v_cmp_eq_u32 vcc, s48, v40\n\t"
    "s_cbranch_vccnz 66f\n\t"
    "s_waitcnt lgkmcnt(0)\n\t"
    "s_barrier\n\t"
    "ds_read_b128 v[104:107], v44 offset:8192\n\t"
    "ds_read_b128 v[108:111], v44 offset:9216\n\t"
    "ds_read_b128 v[112:115], v45 offset:8192\n\t"
    "ds_read_b128 v[116:119], v45 offset:9216\n\t"
    "ds_read_b128 v[120:123], v46 offset:8192\n\t"
    "ds_read_b128 v[124:127], v46 offset:9216\n\t"
    "v_smfmac_f32_16x16x64_bf16 v[48:51], v[100:103], a[8:15], v14\n\t"
    "v_smfmac_f32_16x16x64_bf16 v[56:59], v[100:103], a[136:143], v14\n\t"
    "v_smfmac_f32_16x16x64_bf16 v[52:55], v[100:103], a[72:79], v14\n\t"
    "v_smfmac_f32_16x16x64_bf16 v[60:63], v[100:103], a[200:207], v14\n\t"
    "s_waitcnt lgkmcnt(5)\n\t"
    "v_smfmac_f32_16x16x64_bf16 v[48:51], v[104:107], a[16:23], v14\n\t"
    "v_smfmac_f32_16x16x64_bf16 v[56:59], v[104:107], a[144:151], v14\n\t"
    "v_smfmac_f32_16x16x64_bf16 v[52:55], v[104:107], a[80:87], v14\n\t"
    "ds_read_b128 v[72:75], v13 offset:0\n\t"
    "v_smfmac_f32_16x16x64_bf16 v[60:63], v[104:107], a[208:215], v14\n\t"
    "s_waitcnt lgkmcnt(5)\n\t"
    "v_smfmac_f32_16x16x64_bf16 v[48:51], v[108:111], a[24:31], v14\n\t"
    "buffer_load_dword v15, v12, %[rk], s50 offen offset:0 sc1\n\t"
    "v_smfmac_f32_16x16x64_bf16 v[56:59], v[108:111], a[152:159], v14\n\t"
    "v_smfmac_f32_16x16x64_bf16 v[52:55], v[108:111], a[88:95], v14\n\t"
    "ds_read_b128 v[76:79], v13 offset:4096\n\t"
    "v_smfmac_f32_16x16x64_bf16 v[60:63], v[108:111], a[216:223], v14\n\t"
    "s_waitcnt lgkmcnt(5)\n\t"
    "v_smfmac_f32_16x16x64_bf16 v[48:51], v[112:115], a[32:39], v14\n\t"
    "buffer_load_dword v15, off, %[rc], s55 sc1\n\t"
    "v_smfmac_f32_16x16x64_bf16 v[56:59], v[112:115], a[160:167], v14\n\t"
    "v_smfmac_f32_16x16x64_bf16 v[52:55], v[112:115], a[96:103], v14\n\t"
    "ds_read_b128 v[80:83], v13 offset:1024\n\t"
    "v_smfmac_f32_16x16x64_bf16 v[60:63], v[112:115], a[224:231], v14\n\t"
    "s_waitcnt lgkmcnt(5)\n\t"
    "v_smfmac_f32_16x16x64_bf16 v[48:51], v[116:119], a[40:47], v14\n\t"
    "buffer_store_dword v28, v3, %[rw], s47 offen offset:0 sc1\n\t"
    "v_smfmac_f32_16x16x64_bf16 v[56:59], v[116:119], a[168:175], v14\n\t"
    "v_smfmac_f32_16x16x64_bf16 v[52:55], v[116:119], a[104:111], v14\n\t"
    "ds_read_b128 v[84:87], v13 offset:5120\n\t"
    "v_smfmac_f32_16x16x64_bf16 v[60:63], v[116:119], a[232:239], v14\n\t"
    "s_waitcnt lgkmcnt(5)\n\t"
    "v_smfmac_f32_16x16x64_bf16 v[48:51], v[120:123], a[48:55], v14\n\t"
    "buffer_store_dwordx4 v[16:19], v4, %[rk], s45 offen offset:0 sc1\n\t"
    "v_smfmac_f32_16x16x64_bf16 v[56:59], v[120:123], a[176:183], v14\n\t"
    "v_smfmac_f32_16x16x64_bf16 v[52:55], v[120:123], a[112:119], v14\n\t"
    "ds_read_b128 v[88:91], v13 offset:2048\n\t"
    "v_smfmac_f32_16x16x64_bf16 v[60:63], v[120:123], a[240:247], v14\n\t"
    "s_waitcnt lgkmcnt(5)\n\t"
    "v_smfmac_f32_16x16x64_bf16 v[48:51], v[124:127], a[56:63], v14\n\t"
    "buffer_store_dwordx2 v[30:31], off, %[rc], s54 sc1\n\t"
    "v_smfmac_f32_16x16x64_bf16 v[56:59], v[124:127], a[184:191], v14\n\t"
    "v_smfmac_f32_16x16x64_bf16 v[52:55], v[124:127], a[120:127], v14\n\t"
    "ds_read_b128 v[92:95], v13 offset:6144\n\t"
    "v_smfmac_f32_16x16x64_bf16 v[60:63], v[124:127], a[248:255], v14\n\t"
    "v_smfmac_f32_16x16x64_bf16 v[64:67], v[96:99], v[128:135], v14\n\t"
    "v_smfmac_f32_16x16x64_bf16 v[68:71], v[96:99], v[192:199], v14\n\t"
    "v_smfmac_f32_16x16x64_bf16 v[64:67], v[100:103], v[136:143], v14\n\t"
    "s_nop 1\n\t"
    "v_add_f32 v40, v56, v57\n\t"
    "v_add_f32 v41, v58, v59\n\t"
    "v_cvt_pk_bf16_f32 v16, v40, v41\n\t"
    "v_smfmac_f32_16x16x64_bf16 v[68:71], v[100:103], v[200:207], v14\n\t"
    "v_add_f32 v42, v48, v49\n\t"
    "v_add_f32 v43, v50, v51\n\t"
    "v_cvt_pk_bf16_f32 v46, v42, v43\n\t"
    "ds_write2_b32 v6, v16, v46 offset0:0 offset1:1\n\t"
    "v_add_f32 v40, v60, v61\n\t"
    "v_add_f32 v41, v62, v63\n\t"
    "v_cvt_pk_bf16_f32 v17, v40, v41\n\t"
    "v_add_f32 v42, v52, v53\n\t"
    "v_add_f32 v43, v54, v55\n\t"
    "v_cvt_pk_bf16_f32 v47, v42, v43\n\t"
    "ds_write2_b32 v6, v17, v47 offset0:128 offset1:129\n\t"
    "v_smfmac_f32_16x16x64_bf16 v[64:67], v[104:107], v[144:151], v14\n\t"
    "ds_read_b128 v[96:99], v13 offset:3072\n\t"
    "v_smfmac_f32_16x16x64_bf16 v[68:71], v[104:107], v[208:215], v14\n\t"
    "ds_read_b128 v[100:103], v13 offset:7168\n\t"
    "v_smfmac_f32_16x16x64_bf16 v[64:67], v[108:111], v[152:159], v14\n\t"
    "v_smfmac_f32_16x16x64_bf16 v[68:71], v[108:111], v[216:223], v14\n\t"
    "v_smfmac_f32_16x16x64_bf16 v[64:67], v[112:115], v[160:167], v14\n\t"
    "v_smfmac_f32_16x16x64_bf16 v[68:71], v[112:115], v[224:231], v14\n\t"
    ".p2align 5\n\t"
    "s_waitcnt lgkmcnt(2)\n\t"
    "s_barrier\n\t"
    "ds_read_b64_tr_b16 v[48:49], v7 offset:0\n\t"
    "ds_read_b64_tr_b16 v[50:51], v7 offset:512\n\t"
    "ds_read_b64_tr_b16 v[52:53], v7 offset:1024\n\t"
    "ds_read_b64_tr_b16 v[54:55], v7 offset:1536\n\t"
    "v_smfmac_f32_16x16x64_bf16 v[64:67], v[116:119], v[168:175], v14\n\t"
    "v_smfmac_f32_16x16x64_bf16 v[68:71], v[116:119], v[232:239], v14\n\t"
    "ds_read_b64_tr_b16 v[56:57], v7 offset:2048\n\t"
    "ds_read_b64_tr_b16 v[58:59], v7 offset:2560\n\t"
    "ds_read_b64_tr_b16 v[60:61], v7 offset:3072\n\t"
    "ds_read_b64_tr_b16 v[62:63], v7 offset:3584\n\t"
    "v_smfmac_f32_16x16x64_bf16 v[64:67], v[120:123], v[176:183], v14\n\t"
    "v_smfmac_f32_16x16x64_bf16 v[68:71], v[120:123], v[240:247], v14\n\t"
    "s_waitcnt lgkmcnt(6)\n\t"
    "v_mfma_f32_16x16x32_bf16 v[40:43], v[48:51], v[48:51], 0\n\t"
    "ds_read_b128 v[108:111], v10 offset:0\n\t"
    "v_mfma_f32_16x16x32_bf16 v[44:47], v[72:75], v[48:51], 0\n\t"
    "ds_read_b128 v[112:115], v10 offset:1024\n\t"
    "v_mfma_f32_16x16x32_bf16 v[104:107], v[76:79], v[48:51], 0\n\t"
    "s_waitcnt lgkmcnt(6)\n\t"
    "v_mfma_f32_16x16x32_bf16 v[40:43], v[52:55], v[52:55], v[40:43]\n\t"
    "ds_read_b128 v[72:75], v10 offset:4096\n\t"
    "v_mfma_f32_16x16x32_bf16 v[44:47], v[80:83], v[52:55], v[44:47]\n\t"
    "ds_read_b128 v[76:79], v10 offset:5120\n\t"
    "v_mfma_f32_16x16x32_bf16 v[104:107], v[84:87], v[52:55], v[104:107]\n\t"
    "s_waitcnt lgkmcnt(6)\n\t"
    "v_mfma_f32_16x16x32_bf16 v[40:43], v[56:59], v[56:59], v[40:43]\n\t"
    "ds_read_b128 v[80:83], v10 offset:8192\n\t"
    "v_mfma_f32_16x16x32_bf16 v[44:47], v[88:91], v[56:59], v[44:47]\n\t"
    "ds_read_b128 v[84:87], v10 offset:9216\n\t"
    "v_mfma_f32_16x16x32_bf16 v[104:107], v[92:95], v[56:59], v[104:107]\n\t"
    "s_waitcnt lgkmcnt(6)\n\t"
    "v_mfma_f32_16x16x32_bf16 v[40:43], v[60:63], v[60:63], v[40:43]\n\t"
    "ds_read_b128 v[88:91], v10 offset:12288\n\t"
    "v_mfma_f32_16x16x32_bf16 v[44:47], v[96:99], v[60:63], v[44:47]\n\t"
    "ds_read_b128 v[92:95], v10 offset:13312\n\t"
    "v_mfma_f32_16x16x32_bf16 v[104:107], v[100:103], v[60:63], v[104:107]\n\t"
    "v_smfmac_f32_16x16x64_bf16 v[64:67], v[124:127], v[184:191], v14\n\t"
    "v_smfmac_f32_16x16x64_bf16 v[68:71], v[124:127], v[248:255], v14\n\t"
    "ds_read_b128 v[96:99], v10 offset:6144\n\t"
    "ds_read_b128 v[100:103], v10 offset:7168\n\t"
    "s_nop 1\n\t"
    "v_max3_f32 v42, v40, v41, v44\n\t"
    "v_max3_f32 v43, v45, v46, v47\n\t"
    "v_max3_f32 v124, v104, v105, v106\n\t"
    "v_max3_f32 v42, v42, v43, v124\n\t"
    "v_max_f32 v42, v42, v107\n\t"
    "v_mov_b32 v43, v42\n\t"
    "ds_read_b128 v[48:51], v10 offset:10240\n\t"
    "v_add_f32 v124, v64, v65\n\t"
    "v_permlane16_swap_b32 v42, v43\n\t"
    "v_max_f32 v42, v42, v43\n\t"
    "v_mov_b32 v43, v42\n\t"
    "ds_read_b128 v[52:55], v10 offset:11264\n\t"
    "v_add_f32 v125, v66, v67\n\t"
    "v_permlane32_swap_b32 v42, v43\n\t"
    "v_max_f32 v42, v42, v43\n\t"
    "v_pk_add_f32 v[40:41], v[40:41], v[42:43] op_sel_hi:[1,0] neg_lo:[0,1] neg_hi:[0,1]\n\t"
    "v_pk_add_f32 v[44:45], v[44:45], v[42:43] op_sel_hi:[1,0] neg_lo:[0,1] neg_hi:[0,1]\n\t"
    "v_pk_add_f32 v[46:47], v[46:47], v[42:43] op_sel_hi:[1,0] neg_lo:[0,1] neg_hi:[0,1]\n\t"
    "v_pk_add_f32 v[104:105], v[104:105], v[42:43] op_sel_hi:[1,0] neg_lo:[0,1] neg_hi:[0,1]\n\t"
    "v_pk_add_f32 v[106:107], v[106:107], v[42:43] op_sel_hi:[1,0] neg_lo:[0,1] neg_hi:[0,1]\n\t"
    "v_pk_mul_f32 v[40:41], v[40:41], s[58:59] op_sel_hi:[1,0]\n\t"
    "v_pk_mul_f32 v[44:45], v[44:45], s[58:59] op_sel_hi:[1,0]\n\t"
    "v_pk_mul_f32 v[46:47], v[46:47], s[58:59] op_sel_hi:[1,0]\n\t"
    "v_pk_mul_f32 v[104:105], v[104:105], s[58:59] op_sel_hi:[1,0]\n\t"
    "v_pk_mul_f32 v[106:107], v[106:107], s[58:59] op_sel_hi:[1,0]\n\t"
    "v_exp_f32 v40, v40\n\t"
    "v_exp_f32 v41, v41\n\t"
    "v_cvt_pk_bf16_f32 v18, v124, v125\n\t"
    "v_add_f32 v124, v68, v69\n\t"
    "v_add_f32 v125, v70, v71\n\t"
    "v_cvt_pk_bf16_f32 v19, v124, v125\n\t"
    "v_cvt_pk_bf16_f32 v24, v40, v41\n\t"
    "v_cvt_pk_bf16_f32 v27, v40, v41\n\t"
    "v_exp_f32 v44, v44\n\t"
    "v_exp_f32 v45, v45\n\t"
    "v_mfma_f32_16x16x16_bf16 v[124:127], v[20:21], v[24:25], 0\n\t"
    "v_exp_f32 v46, v46\n\t"
    "v_mfma_f32_16x16x16_bf16 v[64:67], v[18:19], v[24:25], 0\n\t"
    "v_exp_f32 v47, v47\n\t"
    "v_mfma_f32_16x16x16_bf16 v[68:71], v[18:19], v[26:27], 0\n\t"
    "v_exp_f32 v104, v104\n\t"
    "ds_read_b128 v[56:59], v10 offset:14336\n\t"
    "v_exp_f32 v105, v105\n\t"
    "ds_read_b128 v[60:63], v10 offset:15360\n\t"
    "v_exp_f32 v106, v106\n\t"
    "ds_read_b128 v[116:119], v10 offset:2048\n\t"
    "v_exp_f32 v107, v107\n\t"
    "s_waitcnt lgkmcnt(14)\n\t"
    "ds_read_b128 v[120:123], v10 offset:3072\n\t"
    "v_cvt_pk_bf16_f32 v44, v44, v45\n\t"
    "v_cvt_pk_bf16_f32 v45, v46, v47\n\t"
    "v_cvt_pk_bf16_f32 v46, v104, v105\n\t"
    "v_cvt_pk_bf16_f32 v47, v106, v107\n\t"
    "s_nop 1\n\t"
    "v_mfma_f32_16x16x32_bf16 v[124:127], v[20:23], v[44:47], v[124:127]\n\t"
    "v_mfma_f32_16x16x32_bf16 v[64:67], v[32:35], v[44:47], v[64:67]\n\t"
    "v_mfma_f32_16x16x32_bf16 v[68:71], v[36:39], v[44:47], v[68:71]\n\t"
    "v_mov_b64 v[28:29], 0\n\t"
    "v_mov_b64 v[30:31], 0\n\t"
    "s_nop 3\n\t"
    "v_rcp_f32 v42, v124\n\t"
    "s_nop 0\n\t"
    "v_pk_mul_f32 v[64:65], v[64:65], v[42:43] op_sel_hi:[1,0]\n\t"
    "v_pk_mul_f32 v[66:67], v[66:67], v[42:43] op_sel_hi:[1,0]\n\t"
    "v_cvt_pk_bf16_f32 v40, v64, v65\n\t"
    "v_cvt_pk_bf16_f32 v41, v66, v67\n\t"
    "v_pk_mul_f32 v[68:69], v[68:69], v[42:43] op_sel_hi:[1,0]\n\t"
    "v_pk_mul_f32 v[70:71], v[70:71], v[42:43] op_sel_hi:[1,0]\n\t"
    "v_cvt_pk_bf16_f32 v44, v68, v69\n\t"
    "v_cvt_pk_bf16_f32 v45, v70, v71\n\t"
    "s_waitcnt lgkmcnt(14)\n\t"
    "ds_write2_b64 v8, v[40:41], v[44:45] offset0:0 offset1:4\n\t"
    "v_mov_b64 v[124:125], 0\n\t"
    "v_mov_b64 v[126:127], 0\n\t"
    "v_mov_b64 v[64:65], 0\n\t"
    "v_mov_b64 v[66:67], 0\n\t"
    "v_mov_b64 v[68:69], 0\n\t"
    "v_mov_b64 v[70:71], 0\n\t"
    "s_cmp_eq_u32 s40, 63\n\t"
    "s_cbranch_scc1 20f\n\t"
    ".p2align 5\n\t"
    "s_waitcnt lgkmcnt(0)\n\t"
    "s_barrier\n\t"
    "ds_read_b128 v[44:47], v9 offset:0\n\t"
    "ds_read_b128 v[104:107], v9 offset:128\n\t"
    "s_waitcnt lgkmcnt(1)\n\t"
    "v_smfmac_f32_16x16x64_bf16 v[124:127], v[44:47], v[108:115], v14\n\t"
    "v_smfmac_f32_16x16x64_bf16 v[28:31], v[44:47], v[72:79], v14\n\t"
    "v_smfmac_f32_16x16x64_bf16 v[64:67], v[44:47], v[80:87], v14\n\t"
    "v_smfmac_f32_16x16x64_bf16 v[68:71], v[44:47], v[88:95], v14\n\t"
    "s_waitcnt lgkmcnt(0)\n\t"
    "v_smfmac_f32_16x16x64_bf16 v[124:127], v[104:107], v[116:123], v14\n\t"
    "v_smfmac_f32_16x16x64_bf16 v[28:31], v[104:107], v[96:103], v14\n\t"
    "v_smfmac_f32_16x16x64_bf16 v[64:67], v[104:107], v[48:55], v14\n\t"
    "v_smfmac_f32_16x16x64_bf16 v[68:71], v[104:107], v[56:63], v14\n\t"
    "s_nop 4\n\t"
    "v_add_f32 v124, v124, v125\n\t"
    "v_add_f32 v126, v126, v127\n\t"
    "v_cvt_pk_bf16_f32 v40, v124, v126\n\t"
    "v_and_or_b32 v40, v40, -2, s43\n\t"
    "v_add_f32 v28, v28, v29\n\t"
    "v_add_f32 v30, v30, v31\n\t"
    "v_cvt_pk_bf16_f32 v41, v28, v30\n\t"
    "v_and_or_b32 v41, v41, -2, s43\n\t"
    "v_add_f32 v64, v64, v65\n\t"
    "v_add_f32 v66, v66, v67\n\t"
    "v_cvt_pk_bf16_f32 v42, v64, v66\n\t"
    "v_and_or_b32 v42, v42, -2, s43\n\t"
    "v_add_f32 v68, v68, v69\n\t"
    "v_add_f32 v70, v70, v71\n\t"
    "v_cvt_pk_bf16_f32 v43, v68, v70\n\t"
    "v_and_or_b32 v43, v43, -2, s43\n\t"
    "buffer_store_dwordx4 v[40:43], v2, %[rw], 0 offen offset:0 sc1\n\t"
    "s_mov_b32 s47, s41\n\t"
    "s_add_u32 s41, s41, 0x10000\n\t"
    "s_cmp_eq_u32 s41, 0x30000\n\t"
    "s_cselect_b32 s41, 0, s41\n\t"
    "s_cmp_ge_u32 %[w2k], 0x1000\n\t"
    "s_cbranch_scc1 67f\n\t"
    "s_mov_b32 s56, 0x03030303\n\t"
    "s_mov_b32 s57, s56\n\t"
    "v_cndmask_b32 v84, 0, v5, s[56:57]\n\t"
    "s_mov_b32 s56, 0x0c0c0c0c\n\t"
    "s_mov_b32 s57, s56\n\t"
    "v_cndmask_b32 v85, 0, v5, s[56:57]\n\t"
    "s_mov_b32 s56, 0x30303030\n\t"
    "s_mov_b32 s57, s56\n\t"
    "v_cndmask_b32 v86, 0, v5, s[56:57]\n\t"
    "s_mov_b32 s56, 0xc0c0c0c0\n\t"
    "s_mov_b32 s57, s56\n\t"
    "v_cndmask_b32 v87, 0, v5, s[56:57]\n\t"
    "s_nop 15\n\t"
    "s_nop 15\n\t"
    "s_nop 15\n\t"
    "s_nop 15\n\t"
    "s_nop 15\n\t"
    "s_nop 7\n\t"
    "buffer_load_dwordx4 v[48:51], v1, %[rw], 0 offen offset:0 sc1\n\t"
    "buffer_load_dwordx4 v[52:55], v1, %[rw], 0 offen offset:1024 sc1\n\t"
    "s_nop 15\n\t"
    "s_nop 15\n\t"
    "s_nop 7\n\t"
    "buffer_load_dwordx4 v[56:59], v1, %[rw], 0 offen offset:0 sc1\n\t"
    "buffer_load_dwordx4 v[60:63], v1, %[rw], 0 offen offset:1024 sc1\n\t"
    ".p2align 5\n\t"
    "s_waitcnt vmcnt(2)\n\t"
    "v_mfma_f32_16x16x32_bf16 v[88:91], v[48:51], v[84:87], 0\n\t"
    "v_mfma_f32_16x16x32_bf16 v[88:91], v[52:55], v[84:87], v[88:91]\n\t"
    "v_bitop3_b32 v92, v48, v49, s51 bitop3:0x7e\n\t"
    "v_bitop3_b32 v93, v50, v51, s51 bitop3:0x7e\n\t"
    "v_bitop3_b32 v94, v52, v53, s52 bitop3:0x7e\n\t"
    "v_bitop3_b32 v95, v54, v55, s52 bitop3:0x7e\n\t"
    "v_or3_b32 v92, v92, v93, v94\n\t"
    "v_bitop3_b32 v92, v92, v95, 1 bitop3:0xa8\n\t"
    "v_cmp_ne_u32 vcc, 0, v92\n\t"
    "s_cbranch_vccnz 60f\n\t"
    "61:\n\t"
    "v_permlane32_swap_b32 v88, v90\n\t"
    "v_permlane32_swap_b32 v89, v91\n\t"
    "v_add_f32 v88, v88, v90\n\t"
    "v_add_f32 v89, v89, v91\n\t"
    "s_nop 1\n\t"
    "v_permlane16_swap_b32 v88, v89\n\t"
    "v_add_f32 v88, v88, v89\n\t"
    "s_nop 1\n\t"
    "v_mov_b32_dpp v89, v88 quad_perm:[0,1,0,1] row_mask:0xf bank_mask:0xf\n\t"
    "v_mov_b32_dpp v90, v88 quad_perm:[2,3,2,3] row_mask:0xf bank_mask:0xf\n\t"
    "v_cvt_pk_bf16_f32 v88, v89, v90\n\t"
    "v_min_i32 v88, 0x7ffffffe, v88\n\t"
    "buffer_store_dword v88, v3, %[rw], s41 offen offset:0 sc1\n\t"
    "63:\n\t"
    "s_barrier\n\t"
    "v_readfirstlane_b32 s54, v15\n\t"
    "s_cmp_lg_u32 s54, %[t]\n\t"
    "s_cbranch_scc1 64f\n\t"
    "65:\n\t"
    "s_add_u32 s54, s40, 2\n\t"
    "s_lshr_b32 s55, s54, 2\n\t"
    "s_add_u32 s55, s55, 3\n\t"
    "s_min_u32 s55, s55, s54\n\t"
    "s_lshl_b32 s55, s55, 2\n\t"
    "s_add_u32 s55, s55, %[kf]\n\t"
    "s_add_u32 s40, s40, 1\n\t"
    "s_xor_b32 s43, s43, 1\n\t"
    "s_xor_b32 s51, s51, 1\n\t"
    "s_xor_b32 s52, s52, 1\n\t"
    "s_add_u32 s45, s42, %[mq]\n\t"
    "s_add_u32 s42, s42, 0x8000\n\t"
    "s_add_u32 s44, s44, 0x8000\n\t"
    "s_add_u32 s50, s50, 0x8000\n\t"
    "s_nop 15\n\t"
    "s_nop 15\n\t"
    "s_waitcnt vmcnt(1)\n\t"
    "s_branch 10b\n\t"
    "66:\n\t"
    "buffer_load_dwordx4 v[96:99], v0, %[rw], s41 offen sc1\n\t"
    "buffer_load_dwordx4 v[100:103], v0, %[rw], s41 offen offset:1024 sc1\n\t"
    "s_nop 7\n\t"
    "v_mov_b64 v[48:49], 0\n\t"
    "v_mov_b64 v[50:51], 0\n\t"
    "v_mov_b64 v[52:53], 0\n\t"
    "v_mov_b64 v[54:55], 0\n\t"
    "v_mov_b64 v[56:57], 0\n\t"
    "v_mov_b64 v[58:59], 0\n\t"
    "v_mov_b64 v[60:61], 0\n\t"
    "v_mov_b64 v[62:63], 0\n\t"
    "s_sub_i32 s46, s46, 1\n\t"
    "s_cmp_lt_i32 s46, 0\n\t"
    "s_cbranch_scc0 62b\n\t"
    "s_branch 30f\n\t"
    "67:\n\t"
    "s_waitcnt vmcnt(0)\n\t"
    "s_branch 63b\n\t"
    "60:\n\t"
    "buffer_load_dwordx4 v[48:51], v1, %[rw], 0 offen offset:0 sc1\n\t"
    "buffer_load_dwordx4 v[52:55], v1, %[rw], 0 offen offset:1024 sc1\n\t"
    "s_waitcnt vmcnt(2)\n\t"
    "v_mfma_f32_16x16x32_bf16 v[88:91], v[56:59], v[84:87], 0\n\t"
    "v_mfma_f32_16x16x32_bf16 v[88:91], v[60:63], v[84:87], v[88:91]\n\t"
    "v_bitop3_b32 v92, v56, v57, s51 bitop3:0x7e\n\t"
    "v_bitop3_b32 v93, v58, v59, s51 bitop3:0x7e\n\t"
    "v_bitop3_b32 v94, v60, v61, s52 bitop3:0x7e\n\t"
    "v_bitop3_b32 v95, v62, v63, s52 bitop3:0x7e\n\t"
    "v_or3_b32 v92, v92, v93, v94\n\t"
    "v_bitop3_b32 v92, v92, v95, 1 bitop3:0xa8\n\t"
    "v_cmp_ne_u32 vcc, 0, v92\n\t"
    "s_cbranch_vccz 61b\n\t"
    "buffer_load_dwordx4 v[56:59], v1, %[rw], 0 offen offset:0 sc1\n\t"
    "buffer_load_dwordx4 v[60:63], v1, %[rw], 0 offen offset:1024 sc1\n\t"
    "s_waitcnt vmcnt(2)\n\t"
    "v_mfma_f32_16x16x32_bf16 v[88:91], v[48:51], v[84:87], 0\n\t"
    "v_mfma_f32_16x16x32_bf16 v[88:91], v[52:55], v[84:87], v[88:91]\n\t"
    "v_bitop3_b32 v92, v48, v49, s51 bitop3:0x7e\n\t"
    "v_bitop3_b32 v93, v50, v51, s51 bitop3:0x7e\n\t"
    "v_bitop3_b32 v94, v52, v53, s52 bitop3:0x7e\n\t"
    "v_bitop3_b32 v95, v54, v55, s52 bitop3:0x7e\n\t"
    "v_or3_b32 v92, v92, v93, v94\n\t"
    "v_bitop3_b32 v92, v92, v95, 1 bitop3:0xa8\n\t"
    "v_cmp_ne_u32 vcc, 0, v92\n\t"
    "s_cbranch_vccz 61b\n\t"
    "s_sub_i32 s46, s46, 1\n\t"
    "s_cmp_lt_i32 s46, 0\n\t"
    "s_cbranch_scc0 60b\n\t"
    "s_branch 30f\n\t"
    "64:\n\t"
    "buffer_load_dword v15, off, %[rc], s55 sc1\n\t"
    "s_sub_i32 s46, s46, 1\n\t"
    "s_cmp_lt_i32 s46, 0\n\t"
    "s_cbranch_scc1 30f\n\t"
    "s_waitcnt vmcnt(0)\n\t"
    "v_readfirstlane_b32 s54, v15\n\t"
    "s_cmp_lg_u32 s54, %[t]\n\t"
    "s_cbranch_scc1 64b\n\t"
    "s_branch 65b\n\t"
    "20:\n\t"
    "s_add_u32 s45, s42, %[mq]\n\t"
    "v_mov_b32 v30, %[t]\n\t"
    "v_mov_b32 v31, %[t2]\n\t"
    "v_min_i32 v16, 0x7ffffffe, v16\n\t"
    "v_min_i32 v17, 0x7ffffffe, v17\n\t"
    "v_min_i32 v18, 0x7ffffffe, v18\n\t"
    "v_min_i32 v19, 0x7ffffffe, v19\n\t"
    "buffer_store_dwordx4 v[16:19], v4, %[rk], s45 offen sc1\n\t"
    "s_waitcnt vmcnt(1)\n\t"
    "buffer_store_dwordx2 v[30:31], off, %[rc], s54 offset:32 sc1\n\t"
    "s_branch 31f\n\t"
    "30:\n\t"
    "v_mov_b32 v92, %[t]\n\t"
    "buffer_store_dword v92, off, %[rc], 0 offset:256 sc1\n\t"
    "31:\n\t"
    "s_waitcnt vmcnt(0) lgkmcnt(0)\n\t"
      : "+{v[0:31]}"(cv)
      : [rw] "s"(rw), [rk] "s"(rk), [w2k] "s"(w * 2048), [tg] "s"(h >> 4), [rc] "s"(rc), [pf] "s"(hc ? 0x40000000 : CT_PROG + h * 2048 + w * 8 - 64), [mq] "s"(hc ? 0x40000000 : AB_M), [t] "s"(p.tag), [t2] "s"(p.tag ^ TAG2), [kf] "s"(CT_KV + h * 128),
        "{a[0:31]}"(wq[0]), "{a[32:63]}"(wq[1]), "{a[64:95]}"(wq[2]), "{a[96:127]}"(wq[3]),
        "{a[128:159]}"(wk[0]), "{a[160:191]}"(wk[1]), "{a[192:223]}"(wk[2]), "{a[224:255]}"(wk[3]),
        "{v[128:159]}"(wv[0]), "{v[160:191]}"(wv[1]), "{v[192:223]}"(wv[2]), "{v[224:255]}"(wv[3])
      : "memory", "vcc", "scc", CLOBBER_V32_127, CLOBBER_S40_63);
}

template <int NB>
__device__ __forceinline__ void attn_mem(v4u (&mq)[NB][4], __amdgpu_buffer_rsrc_t rd, u32 lds, int ab, bool last) {
  const int lane = lane_id(), g = lane >> 4, n = lane & 15;
  for (u32 spins = 0; spins < SPIN_CAP; ++spins) {
    asm volatile("" ::: "memory");
#pragma unroll
    for (int i = 0; i < 4 * NB; ++i) mq[i >> 2][i & 3] = ld16(rd, ab + (i >> 2) * NSEG * AB_BLK + AB_M + (i & 3) * 2048);
    __builtin_amdgcn_sched_barrier(0);
    if (!last || (all_there(mq[0]) && all_there(mq[NB - 1]))) break;
  }
#pragma unroll
  for (int i = 0; i < 4 * NB; ++i) {
    *(LAS u32*)(lds + (32 * i + n) * 32 + g * 8) = mq[i >> 2][i & 3].x;
    *(LAS u32*)(lds + (32 * i + 16 + n) * 32 + g * 8) = mq[i >> 2][i & 3].y;
  }
}
__device__ __forceinline__ void attn_tile(v4u (&o)[4], const v4u* Q, f32x4 t1, f32x4 t2, const v4u (&V)[8], const v4u (&mq)[4], u32 lds, bool qt) {
  const int lane = lane_id(), g = lane >> 4, n = lane & 15;
  f32x4 t0 = {0.f, 0.f, 0.f, 0.f};
#pragma unroll
  for (int ks = 0; ks < 4; ++ks) {
    const u32 ka = lds + (32 * ks + 4 * g + (n >> 2)) * 32 + (n & 3) * 8;
    const v2u klo = trread(ka), khi = trread(ka + 512);
    t0 = mfma32(v4u{klo.x, klo.y, khi.x, khi.y}, Q[ks], t0);
  }
  float v[10];
#pragma unroll
  for (int r = 0; r < 4; ++r) {
    const bool le = 4 * g + r <= n;
    if (r < 2) v[8 + r] = t0[r] * SCALE_LOG2E;
    v[r] = (qt || le) ? t1[r] * SCALE_LOG2E : MASKED * 1.4426950408889634f;
    v[4 + r] = (qt && le) ? t2[r] * SCALE_LOG2E : MASKED * 1.4426950408889634f;
  }
  float mx = v[0];
#pragma unroll
  for (int i = 1; i < 10; ++i) mx = fmaxf(mx, v[i]);
  mx = max_g(mx);
  float ls = 0.f;
#pragma unroll
  for (int i = 0; i < 10; ++i) {
    v[i] = __builtin_amdgcn_exp2f(v[i] - mx);
    ls += v[i];
  }
  const float iv = 1.0f / sum_g(ls);
  const v4u pt = {pk2(v[0], v[1]), pk2(v[2], v[3]), pk2(v[4], v[5]), pk2(v[6], v[7])};
  const u32 pm = pk2(v[8], v[9]);
#pragma unroll
  for (int e2 = 0; e2 < 4; ++e2) {
    const v4u vm4 = {mq[e2].z, mq[e2].w, 0u, 0u};
    f32x4 a = {0.f, 0.f, 0.f, 0.f}, b = a;
    a = mfma32(V[2 * e2], pt, a); a = mfma32(vm4, v4u{pm, 0u, 0u, 0u}, a);
    b = mfma32(V[2 * e2 + 1], pt, b); b = mfma32(vm4, v4u{0u, pm, 0u, 0u}, b);
    o[e2] = v4u{pk2(a[0] * iv, a[1] * iv), pk2(a[2] * iv, a[3] * iv), pk2(b[0] * iv, b[1] * iv), pk2(b[2] * iv, b[3] * iv)};
  }
}
__device__ __forceinline__ bool attn_ready(const Ctx& p, int s, int h) {
  const bool ok = wait_set<false>(p, CT_KV + h * 128 + seg_word(s) * 4, 1) && wait_set<false>(p, CT_KV + (32 + (h >> 1)) * 128 + seg_word(s) * 4, 1);
  __builtin_amdgcn_fence(__ATOMIC_ACQUIRE, "wavefront");
  return ok;
}
__device__ __forceinline__ bool attn_wait(const Ctx& p, int s, int h) {
  const bool ok = wait_set<true>(p, CT_PROG + h * 2048 + (s >= SLAST ? min(s - 1, NSEG - 3) : s) * 32, 4);
  __builtin_amdgcn_fence(__ATOMIC_ACQUIRE, "wavefront");
  return ok;
}
__device__ __forceinline__ bool attn_item(const Ctx& p, __amdgpu_buffer_rsrc_t rd, u32 lds  , int s, int h) {
  const int lane = lane_id();
  const int ab = (h * NSEG + s) * AB_BLK + lane * 16;
  if (!attn_ready(p, s, h)) return false;
  v4u KF[8], QF[8], VF[8];
#pragma unroll
  for (int i = 0; i < 8; ++i) { KF[i] = ld16(rd, ab + AB_K + i * 1024); QF[i] = ld16(rd, ab + AB_Q + i * 1024); }
#pragma unroll
  for (int i = 0; i < 8; ++i) VF[i] = ld16(rd, ab + AB_V + i * 1024);
  f32x4 t01 = {0.f, 0.f, 0.f, 0.f}, t11 = t01, t12 = t01;
#pragma unroll
  for (int ks = 0; ks < 4; ++ks) {
    t01 = mfma32(KF[ks], QF[ks], t01);
    t11 = mfma32(KF[ks], QF[4 + ks], t11);
    t12 = mfma32(KF[4 + ks], QF[4 + ks], t12);
  }
  if (!attn_wait(p, s, h)) return false;
  v4u mq[1][4], o[4];
  attn_mem<1>(mq, rd, lds, ab, s >= SLAST);
#pragma unroll
  for (int qt = 0; qt < 2; ++qt) {
    attn_tile(o, QF + 4 * qt, qt ? t11 : t01, t12, VF, mq[0], lds, qt);
#pragma unroll
    for (int e2 = 0; e2 < 4; ++e2) st16(rd, RD_OF + ((2 * s + qt) * 128 + 4 * h + e2) * 1024 + lane * 16, unsent(o[e2]));
  }
  if (s < SLAST) WAIT_VM(0);
  if (lane == 0) __hip_atomic_store((gu64*)ctw(p, CT_O + s * 256 + h * 8), tag64(p), RLX_AGENT);
  return true;
}

template <int NKS>
__device__ __forceinline__ void load_wcols(const Ctx& p, v4u (&wf)[NKS][4], u32 lds, const u16* src  ) {
  const int lane = lane_id(), w = p.w, g = lane >> 4, n = lane & 15;
  const int f = (lane >> 3) & 3;
  src += (size_t)(lane >> 3) * DM + (((lane & 7) ^ (f << 1)) << 3);
  const u32 a = lds + w * 32768 + (4 * g + (n >> 2)) * 128 + (n & 3) * 8;
#pragma unroll
  for (int b = 0; b < NKS; b += 8) {
    const int nb = NKS - b < 8 ? NKS - b : 8;
#pragma unroll 4
    for (int i = 0; i < nb * 4; ++i) glds16(src + (size_t)(b * 4 + i) * 8 * DM, lds + w * 32768 + i * 1024);
    WAIT_VM(0);
#pragma unroll
    for (int i = 0; i < nb; ++i)
#pragma unroll
      for (int t = 0; t < 4; ++t) {
        const u32 at = a + ((t ^ (n >> 2)) << 5) + i * 4096;
        const v2u lo = trread(at), hi = trread(at + 2048);
        wf[b + i][t] = v4u{lo.x, lo.y, hi.x, hi.y};
      }
    WAIT_LGKM0();
  }
  BARRIER();
}
template <bool WIDE>
__device__ __forceinline__ bool wg_wait(const Ctx& p, LAS u32* word, int off, int n) {
  if (p.w == 0) {
    const bool ok = wait_set<WIDE>(p, off, n);
    *word = ok;
    WAIT_LGKM0();
  }
  BARRIER();
  __builtin_amdgcn_fence(__ATOMIC_ACQUIRE, "wavefront");
  return *word != 0u;
}
template <int NKS>
__device__ __forceinline__ void tile_product(const Ctx& p, const v4u (&wf)[NKS][4], const v4u (&af)[NKS], u32 lds, int tt) {
  LAS f32x4* red = (LAS f32x4*)lds + (p.w * 8 + tt * 4) * 64 + lane_id();
  f32x4 c[4];
#pragma unroll
  for (int t = 0; t < 4; ++t) c[t] = f32x4{0.f, 0.f, 0.f, 0.f};
#pragma unroll
  for (int i = 0; i < NKS; ++i)
#pragma unroll
    for (int t = 0; t < 4; ++t) c[t] = mfma32(wf[i][t], af[i], c[t]);
#pragma unroll
  for (int t = 0; t < 4; ++t) red[t * 64] = c[t];
}
__device__ __forceinline__ void tile_sum(const Ctx& p, f32x4 (&part)[2], u32 lds) {
  const LAS f32x4* red = (const LAS f32x4*)lds + 2 * p.w * 64 + lane_id();
  WAIT_LGKM0();
  BARRIER();
#pragma unroll
  for (int i = 0; i < 2; ++i) part[i] = (red[i * 64] + red[512 + i * 64]) + (red[1024 + i * 64] + red[1536 + i * 64]);
}
template <int NKS>
__device__ __forceinline__ void seg_product(const Ctx& p, f32x4 (&part)[2], const v4u (&wf)[NKS][4], __amdgpu_buffer_rsrc_t rd, u32 lds, int off, int tt_stride, bool check) {
  const int lane = lane_id();
  v4u af[2][NKS];
  for (u32 spins = 0; spins < SPIN_CAP; ++spins) {
    asm volatile("" ::: "memory");
#pragma unroll
    for (int tt = 0; tt < 2; ++tt)
#pragma unroll
      for (int i = 0; i < NKS; ++i) af[tt][i] = ld16(rd, off + tt * tt_stride + i * 1024 + lane * 16);
    __builtin_amdgcn_sched_barrier(0);
    if (!check || (all_there(af[0]) && all_there(af[1]))) break;
  }
  tile_product<NKS>(p, wf, af[0], lds, 0);
  tile_product<NKS>(p, wf, af[1], lds, 1);
  tile_sum(p, part, lds);
}
constexpr int SFIRST = 12;
__device__ __forceinline__ void oproj_role(const Ctx& p, __amdgpu_buffer_rsrc_t rd, u32 lds, int kq, int ng, int set) {
  const int lane = lane_id(), w = p.w, ks0 = kq * 32 + w * 8;
  if (set == 2) wait_set<true, 32>(p, CT_PROG, 1);
  v4u wf[8][4];
  load_wcols<8>(p, wf, lds, p.wo + (size_t)ks0 * 32 * DM + 64 * ng);
  LAS u32* word = (LAS u32*)(lds + 32768);
  const int slab = RD_SL + (ng * 4 + kq) * 8192 + w * 2048 + lane * 16, tw = CT_T + ng * 32 + kq * 8;
  f32x4 part[2];
  auto units = [&](int s0, int s1, int step) {
    for (int s = s0; s < s1; s += step) {
      if (!wg_wait<true>(p, word, CT_O + s * 256 + kq * 64, 8)) return;
      seg_product<8>(p, part, wf, rd, lds, RD_OF + (2 * s * 128 + ks0) * 1024, 128 * 1024, s >= SLAST);
#pragma unroll
      for (int i = 0; i < 2; ++i) st16(rd, slab + s * 262144 + i * 1024, unsent(__builtin_bit_cast(v4u, part[i])));
      publish<true>(p, tw + s * 256, s < SLAST);
    }
  };
  if (set == 2) wait_set<true, 64>(p, CT_O, 1);
  units(set < 2 ? SFIRST + set : 0, set < 2 ? NSEG - 12 : set == 2 ? SFIRST : 0, set < 2 ? 2 : 1);
  const int s = NSEG - 2 + (set >> 1), qt = set & 1;
  v4u Q[2][4], V[2][8], af[8];
  f32x4 t1[2], t2[2];
#pragma unroll
  for (int hh = 0; hh < 2; ++hh) {
    const int h = kq * 8 + 2 * w + hh, ab = (h * NSEG + s) * AB_BLK + lane * 16;
    attn_ready(p, s, h);
    v4u K[8];
#pragma unroll
    for (int i = 0; i < 8; ++i) { K[i] = ld16(rd, ab + AB_K + i * 1024); V[hh][i] = ld16(rd, ab + AB_V + i * 1024); }
#pragma unroll
    for (int i = 0; i < 4; ++i) Q[hh][i] = ld16(rd, ab + AB_Q + (qt * 4 + i) * 1024);
    t1[hh] = f32x4{0.f, 0.f, 0.f, 0.f}; t2[hh] = t1[hh];
#pragma unroll
    for (int ks = 0; ks < 4; ++ks) { t1[hh] = mfma32(K[ks], Q[hh][ks], t1[hh]); t2[hh] = mfma32(K[4 + ks], Q[hh][ks], t2[hh]); }
  }
  if (set >= 2) wait_set<true, 64>(p, CT_O + (NSEG - 8) * 256, 1);
  units(set < 2 ? NSEG - 12 + set : NSEG - 6 + set, set < 2 ? NSEG - 4 : NSEG - 5 + set, 2);
  if (!attn_wait(p, s, kq * 8 + 2 * w)) return;
  v4u mq[2][4];
  attn_mem<2>(mq, rd, lds + 36864 + w * 8192, ((kq * 8 + 2 * w) * NSEG + s) * AB_BLK + lane * 16, true);
#pragma unroll
  for (int hh = 0; hh < 2; ++hh) {
    v4u o[4];
    attn_tile(o, Q[hh], t1[hh], t2[hh], V[hh], mq[hh], lds + 36864 + w * 8192 + hh * 4096, qt);
#pragma unroll
    for (int i = 0; i < 4; ++i) af[4 * hh + i] = o[i];
  }
  tile_product<8>(p, wf, af, lds, qt);
  tile_sum(p, part, lds);
  if ((w >> 1) == qt) {
#pragma unroll
    for (int i = 0; i < 2; ++i) st16(rd, slab + s * 262144 + i * 1024, unsent(__builtin_bit_cast(v4u, part[i])));
  }
  publish<true>(p, tw + s * 256, false);
}
__device__ __forceinline__ void final_role(const Ctx& p, __amdgpu_buffer_rsrc_t rd, u32 lds, int ng, int s0) {
  const int lane = lane_id(), w = p.w, g = lane >> 4, n = lane & 15;
  v4u wf[14][4];
  load_wcols<14>(p, wf, lds, p.w2 + (size_t)w * 14 * 32 * DM + 64 * ng);
  LAS u32* word = (LAS u32*)(lds + 32768);
  if (!wg_wait<false>(p, word + 2, CT_G, 4)) return;
  for (int s = s0; s < NSEG; s += 4) {
    if (!wg_wait<false>(p, word, CT_H + seg_word(s) * 32, 7)) return;
    f32x4 b[2];
    seg_product<14>(p, b, wf, rd, lds, RD_HF + (2 * s * 56 + w * 14) * 1024, 56 * 1024, false);
    const int tok = s * 32 + (w >> 1) * 16 + n;
    const float gt = __uint_as_float(__builtin_amdgcn_raw_buffer_load_b32(rd, RD_G + tok * 4, 0, SC1)), og = rbf(1.0f - gt);
#pragma unroll
    for (int i = 0; i < 2; ++i)
#pragma unroll
      for (int k = 0; k < 4; ++k) b[i][k] = rbf(og * rbf(b[i][k]));
    if (!wg_wait<true>(p, word + 1, CT_T + (s * 8 + ng) * 32, s < SLAST ? 4 : 1)) return;
    v4u su[8];
    for (u32 spins = 0; spins < SPIN_CAP; ++spins) {
      asm volatile("" ::: "memory");
#pragma unroll
      for (int q = 0; q < 8; ++q) su[q] = ld16(rd, RD_SL + ((s * 8 + ng) * 4 + (q >> 1)) * 8192 + w * 2048 + (q & 1) * 1024 + lane * 16);
      __builtin_amdgcn_sched_barrier(0);
      if (s < SLAST || all_there(su)) break;
    }
    f32x4 sl[4][2];
#pragma unroll
    for (int q = 0; q < 8; ++q) sl[q >> 1][q & 1] = __builtin_bit_cast(f32x4, su[q]);
#pragma unroll
    for (int i = 0; i < 2; ++i) {
      const f32x4 a = (sl[0][i] + sl[1][i]) + (sl[2][i] + sl[3][i]);
      *(v2u*)(p.out + (size_t)tok * DM + ng * 64 + ((w & 1) * 2 + i) * 16 + 4 * g) =
          v2u{pk2(rbf(gt * rbf(a[0])) + b[i][0], rbf(gt * rbf(a[1])) + b[i][1]), pk2(rbf(gt * rbf(a[2])) + b[i][2], rbf(gt * rbf(a[3])) + b[i][3])};
    }
    if (ng == 3) TLS(4096 + 64 + s);
  }
}

__device__ __forceinline__ void gate_role(const Ctx& p, __amdgpu_buffer_rsrc_t rd, int q) {
  const int tid = p.w * 64 + lane_id(), part = tid & 7;
  v4u gw[8];
#pragma unroll
  for (int i = 0; i < 8; ++i) gw[i] = ((const v4u*)p.gw)[i * 8 + part];
#pragma unroll 2
  for (int it = 0; it < 16; ++it) {
    const int tok = q * 512 + it * 32 + (tid >> 3);
    const v4u* xr = (const v4u*)(p.x + (size_t)tok * DM) + part;
    float sacc = 0.f;
#pragma unroll
    for (int i = 0; i < 8; ++i) {
      const v4u a = xr[i * 8], b = gw[i];
#pragma unroll
      for (int d = 0; d < 4; ++d) sacc += rbf(bflo(a[d]) * bflo(b[d])) + rbf(bfhi(a[d]) * bfhi(b[d]));
    }
    sacc += __shfl_xor(sacc, 1); sacc += __shfl_xor(sacc, 2); sacc += __shfl_xor(sacc, 4);
    if (part == 0) __builtin_amdgcn_raw_buffer_store_b32(__float_as_uint(rbf(1.0f / (1.0f + __expf(-rbf(sacc))))), rd, RD_G + tok * 4, 0, SC1);
  }
  publish<false>(p, CT_G + q * 4);
}

__device__ __forceinline__ void init_role(const Ctx& p, __amdgpu_buffer_rsrc_t rd, int q) {
  const __amdgpu_buffer_rsrc_t rs = srd(p.ws + WS_WIN, 2 * MB);
  const int lane = lane_id(), tid = p.w * 64 + lane;
  const v4u s4 = {SENT, SENT, SENT, SENT};
#pragma unroll
  for (int k = 0; k < 4; ++k) {
    const int i = (q * 4 + k) * 256 + tid;
    const u32 t = ((i >> 6) & 1) ^ 1;
    st16(rs, i * 16, v4u{t, t, t, t});
  }
  for (int j = q * 384 + tid; j < (q + 1) * 384; j += 256) {
    v4u v = s4;
    if (j < 4096) {
      const int ks = (j >> 6) & 7, g = (j >> 4) & 3, r = j & 15;
      const u16* m0 = p.mem0 + (r >> 1) * DM + 64 * ks + 32 * (r & 1) + 4 * g;
#pragma unroll
      for (int pq = 0; pq < 4; ++pq) {
        v[pq] = (u32)m0[pq] | ((u32)m0[pq + 16] << 16);
        if (v[pq] == SENT) v[pq] = 0x7FFFFFFEu;
      }
    }
    st16(rs, 2 * WIN_MEM + j * 16, v);
  }
#pragma unroll
  for (int k = 0; k < 2 * (NSEG - SLAST); ++k) {
    const int s = SLAST + (k >> 1), o = ((k & 1) * 256 + tid) * 16;
    st16(rd, (q * NSEG + s) * AB_BLK + AB_M + o, s4);
    st16(rd, RD_OF + (2 * s * 128 + q * 8) * 1024 + o, s4);
    st16(rd, RD_SL + (s * 32 + q) * 8192 + o, s4);
  }
  if (q == 0 && p.w == 0) {
    WAIT_VM(0);
    u32 best[2] = {1u << 30, 1u << 30};
    for (int it = 0; it < 4; ++it)
#pragma unroll
      for (int par = 0; par < 2; ++par) {
        const unsigned long long a = __builtin_amdgcn_s_memtime();
        if (lane < 8) st16(rs, (int)(2 * MB - 32768) + par * 8192 + lane * 16, s4);
        WAIT_VM(0);
        best[par] = min(best[par], (u32)(__builtin_amdgcn_s_memtime() - a));
      }
    if (lane == 0) __hip_atomic_store(ctw(p, CT_PAR + 4), (p.tag ^ TAG2) + (best[1] < best[0] ? 1u : 0u), RLX_AGENT);
  }
  publish<false>(p, CT_INIT + q * 4);
  TLS(4420 + q);
  if (q == 1) {
    wait_set<false, 8>(p, CT_ARR + p.w * 256, 64);
    BARRIER();
    if (tid == 0) __hip_atomic_store(ctw(p, CT_EPOCH), p.tag, RLX_AGENT);
  }
}

constexpr int LDS_MAIN = CH_LDS;
constexpr int CODE_KB = 68;

__global__ __launch_bounds__(256, 1) void k_main(Params pp) {
  Ctx p;
  (Params&)p = pp;
  p.w = __builtin_amdgcn_readfirstlane(threadIdx.x >> 6);
  extern __shared__ __attribute__((aligned(16))) unsigned char smem[];
  const u32 lds = lds_base(smem);
  const u32 id = __builtin_amdgcn_workgroup_id_x();
  p.b = id & 255;
  p.tag = 0;
  p.lab = id >> 16 ? id >> 16 : __builtin_amdgcn_s_getreg((3 << 11) | 20) & 7;
  const int x = p.lab & 7, r = p.b >> 3;
  if (p.w == 3 && r < CODE_KB / 4) {
    asm volatile("s_getpc_b64 s[4:5]\n1:\n\ts_sub_u32 s4, s4, 1b-_Z6k_main6Params\n\ts_subb_u32 s5, s5, 0\n\tglobal_load_dword v255, %0, s[4:5]"
                 :: "v"(r * 4096 + lane_id() * 64) : "memory", "s4", "s5", "scc", "v255");
  }
  TLS(p.b * 8);
  if (x < 4 && r < 16) { chain_role(p, lds, x * 16 + r); return; }
  const __amdgpu_buffer_rsrc_t rd = __builtin_amdgcn_make_buffer_rsrc((void*)(p.ws + WS_AB), 0, RD_SIZE, RSRC_W3);
  LAS u32* keep = (LAS u32*)(lds + LDS_MAIN - 16);
  if (p.lab & 16) p.tag = __builtin_amdgcn_readfirstlane(*keep);
  if (x >= 4 || (p.lab & 16)) {
    const int q = x >= 4 ? (x - 4) * 32 + r : x * 8 + (r & 7);
    proj_role(p, rd, lds, (q >> 2) + (x < 4 ? 48 : (p.lab & 16) * 2), q & 3);
    if (!(p.lab & 16) && x < 6) {
      *keep = p.tag;
      restart(p, x + 16);
    }
  } else {
    const u32 ev = epoch_ask(p);
    WAIT_VM(0);
    BARRIER();
    adopt_tag(p, ev);
  }
  TLS(p.b * 8 + 1);
  if (x < 4 && r < 24 && !(p.lab & 16)) {
    init_role(p, rd, (r - 16) * 4 + x);
    wait_set<true, 32>(p, CT_PROG, 1);
    const int q = x * 8 + (r & 7);
    if (q < 28) {
      *keep = p.tag;
      restart(p, x + 16);
    }
    gate_role(p, rd, q - 28);
  }
  if (x < 6) {
    const int o = x >= 4 ? r : x * 8 + (r & 7);
    oproj_role(p, rd, lds, o >> 3, o & 7, x >= 4 ? x - 4 : r >= 24 ? 2 : 3);
  } else if (x == 6) {
    final_role(p, rd, lds, r & 7, r >> 3);
  } else if (x == 7) {
    const int w = p.w;
    for (int s = w; s < NSEG - 2; s += 4) {
      if (!attn_item(p, rd, lds + w * 4096, s, r)) break;
      if (r == 5) TLS(4096 + s);
    }
  }
  TLS(p.b * 8 + 7);
}

extern "C" void kernel_launch(void* const* d_in, const int* in_sizes, int n_in, void* d_out, int out_size, void* d_ws, size_t ws_size,
                              hipStream_t stream) {
  Params p;
  p.x = (const u16*)d_in[0];
  p.mem0 = (const u16*)d_in[1];
  p.wq = (const u16*)d_in[2];
  p.wk = (const u16*)d_in[3];
  p.wv = (const u16*)d_in[4];
  p.wo = (const u16*)d_in[5];
  p.w1 = (const u16*)d_in[8];
  p.w2 = (const u16*)d_in[9];
  p.gw = (const u16*)d_in[10];
  p.out = (u16*)d_out;
  p.ws = (unsigned char*)(((uintptr_t)d_ws + 2 * MB - 1) & ~(uintptr_t)(2 * MB - 1));
  (void)hipFuncSetAttribute((const void*)k_main, hipFuncAttributeMaxDynamicSharedMemorySize, LDS_MAIN);
  k_main<<<256, 256, LDS_MAIN, stream>>>(p);
}
```
